```python
import torch

def _softmax(x, axis=-1):
    return torch.softmax(x, dim=axis)
def _gelu(x):
    return torch.nn.functional.gelu(x, approximate="tanh")
def _silu(x):
    return torch.nn.functional.silu(x)
def _sigmoid(x):
    return torch.sigmoid(x)
def _layernorm(x, axis=-1, eps=1e-5):
    m = x.mean(dim=axis, keepdim=True)
    v = x.var(dim=axis, keepdim=True, unbiased=False)
    return (x - m) / torch.sqrt(v + eps)
def _scatter_add(tgt, idx, src, axis=0):
    return tgt.clone().index_add_(axis, idx, src)
def _segment_sum(src, seg, n):
    out = torch.zeros((n,) + src.shape[1:], dtype=src.dtype)
    return out.index_add_(0, seg, src)
def _pad(x, pw):
    flat = tuple(v for pair in reversed(pw) for v in pair)
    return torch.nn.functional.pad(x, flat)

kernel_name = 'sequential_preLN__ssm_conv_h3__dense_activation_relu__prompt'

def setup_inputs():

    _w = (lambda a: (lambda t: t.to(torch.bfloat16) if t.is_floating_point() else t)(torch.as_tensor(a)))
    def _A_setup():
        B, S, D = 1, 2048, 512
        ks = 4
        return {
            "q": _w((lambda *s: torch.randn(*s, dtype=torch.bfloat16) * 0.05)(B, S, D)),
            "k": _w((lambda *s: torch.randn(*s, dtype=torch.bfloat16) * 0.4)(B, S, D)),
            "v": _w((lambda *s: torch.randn(*s, dtype=torch.bfloat16) * 0.4)(B, S, D)),
            "shift_k": _w((lambda *s: torch.randn(*s, dtype=torch.bfloat16) * 0.4)(D, ks) * 0.5),
            "ssm_k": _w((lambda *s: torch.randn(*s, dtype=torch.bfloat16) * 0.4)(D, S) * (1.0 / S ** 0.5)),
            "ssm_D": _w((lambda *s: torch.randn(*s, dtype=torch.bfloat16) * 0.4)(D)),
        }
    _A_in = _A_setup()

    def _B_setup():
        B, S, D, Dff = 1, 2048, 512, 1792
        return {
            "x": _w((lambda *s: torch.randn(*s, dtype=torch.bfloat16) * 0.05)(B, S, D)),
            "w1": _w((lambda *s: torch.randn(*s, dtype=torch.bfloat16) * 0.05)(D, Dff)),
            "w2": _w((lambda *s: torch.randn(*s, dtype=torch.bfloat16) * 0.05)(Dff, D)),
        }
    _B_in = _B_setup()

    return {
        "A_q": _A_in["q"],
        "A_k": _A_in["k"],
        "A_v": _A_in["v"],
        "A_shift_k": _A_in["shift_k"],
        "A_ssm_k": _A_in["ssm_k"],
        "A_ssm_D": _A_in["ssm_D"],
        "B_x": _B_in["x"],
        "B_w1": _B_in["w1"],
        "B_w2": _B_in["w2"],
    }

def reference(A_q, A_k, A_v, A_shift_k, A_ssm_k, A_ssm_D, B_x, B_w1, B_w2):
    def _A_ref(q, k, v, shift_k, ssm_k, ssm_D):
        B, S, D = q.shape
        ks = shift_k.shape[-1]
        k_pad = _pad(k, ((0, 0), (ks - 1, 0), (0, 0)))
        k_shift = torch.zeros_like(k)
        for t in range(ks):
            k_shift = k_shift + k_pad[:, t:t + S, :] * shift_k[:, ks - 1 - t][None, None, :]
        kv = k_shift * v
        L = 2 * S
        kv_pad = _pad(kv, ((0, 0), (0, S), (0, 0)))
        kf_pad = _pad(ssm_k, ((0, 0), (0, S)))
        kv_f = (lambda x, axes=(-2,-1): torch.fft.rfft2(x.float(), dim=axes))(kv_pad[:, None, :, :], axes=(1, 2))
        kf_f = (lambda x, axes=(-2,-1): torch.fft.rfft2(x.float(), dim=axes))(kf_pad[:, None, :], axes=(1, 2))
        kf_f = (lambda x, *d: x.permute(*d))(kf_f, 1, 2, 0)
        y_f = kv_f * kf_f[None, :, :, :]
        y = (lambda x, s, axes=(-2,-1): torch.fft.irfft2(x, s=s, dim=axes).to(torch.bfloat16))(y_f, (1, L), axes=(1, 2))[:, 0, :S, :]
        y = y + kv * ssm_D[None, None, :]
        return q * y

    def _B_ref(x, w1, w2):
        h = torch.einsum("bsd,df->bsf", x, w1)
        zero = torch.zeros_like(h)
        h = torch.maximum(h, zero)
        return torch.einsum("bsf,fd->bsd", h, w2)

    x = A_q
    h = x + _A_ref(_layernorm(x, axis=-1), A_k, A_v, A_shift_k, A_ssm_k, A_ssm_D)
    h2 = h + _B_ref(_layernorm(h, axis=-1), B_w1, B_w2)
    return h2
```

```cpp
#include <hip/hip_runtime.h>
#include <hip/hip_bf16.h>
#include <stdint.h>

typedef unsigned short u16;
typedef unsigned int u32;
typedef __bf16 bf16x8 __attribute__((ext_vector_type(8)));
typedef __bf16 bf16x2 __attribute__((ext_vector_type(2)));
typedef short s16x4 __attribute__((ext_vector_type(4)));
typedef float f32x4 __attribute__((ext_vector_type(4)));
typedef float f32x2 __attribute__((ext_vector_type(2)));
typedef u32 u32x4 __attribute__((ext_vector_type(4)));
typedef u32 u32x2 __attribute__((ext_vector_type(2)));
typedef u32x4 u32x4_u __attribute__((aligned(2)));
#define LAS __attribute__((address_space(3)))

constexpr int S = 2048, D = 512, FF = 1792;
constexpr float EPS = 1e-5f;

__device__ __forceinline__ float blo(u32 v) { return __uint_as_float(v << 16); }
__device__ __forceinline__ float bhi(u32 v) { return __uint_as_float(v & 0xffff0000u); }
__device__ __forceinline__ u32 pk2(float lo, float hi) {
  bf16x2 r = {(__bf16)lo, (__bf16)hi};
  return __builtin_bit_cast(u32, r);
}
__device__ __forceinline__ f32x4 mfma16(u32x4 a, u32x4 b, f32x4 c) {
  return __builtin_amdgcn_mfma_f32_16x16x32_bf16(__builtin_bit_cast(bf16x8, a), __builtin_bit_cast(bf16x8, b), c, 0, 0, 0);
}
__device__ __forceinline__ u32 perm_lo(u32 a, u32 b) { return __builtin_amdgcn_perm(b, a, 0x05040100u); }
__device__ __forceinline__ u32 perm_hi(u32 a, u32 b) { return __builtin_amdgcn_perm(b, a, 0x07060302u); }

constexpr size_t WS_KVR = 0;
constexpr size_t WS_XT = WS_KVR + (size_t)D * S * 2;
constexpr size_t WS_ST = WS_XT + (size_t)D * S * 2;
constexpr size_t WS_HT = WS_ST + (size_t)S * 8;
constexpr size_t WS_G = WS_HT + (size_t)D * S * 2;

__global__ __launch_bounds__(256) void k_p1(const u32* __restrict__ x, const u32* __restrict__ k, const u32* __restrict__ v,
                                            const u32x4* __restrict__ shift, char* __restrict__ ws) {
  const int wg = blockIdx.x, cg = wg & 7, tt = wg >> 3;
  const int tid = threadIdx.x, l = tid & 63, w = tid >> 6;
  const int o = l & 7, cp = cg * 32 + w * 8 + (l >> 3);
  const int t0 = tt * 64 + o * 8;
  u32 kk[11], vv[8], xx[8];
#pragma unroll
  for (int i = 0; i < 11; ++i) { int t = t0 - 3 + i; kk[i] = k[(t < 0 ? 0 : t) * (D / 2) + cp]; if (t < 0) kk[i] = 0; }
#pragma unroll
  for (int i = 0; i < 8; ++i) vv[i] = v[(t0 + i) * (D / 2) + cp];
#pragma unroll
  for (int i = 0; i < 8; ++i) xx[i] = x[(t0 + i) * (D / 2) + cp];
  const u32x4 sh = shift[cp];
  u32x4 r0 = ((const u32x4*)x)[(wg * 8 + w * 2) * (D / 8) + l];
  u32x4 r1 = ((const u32x4*)x)[(wg * 8 + w * 2 + 1) * (D / 8) + l];
  float a[8], b[8];
#pragma unroll
  for (int i = 0; i < 8; ++i) {
    float s0 = blo(kk[i + 3]) * blo(sh.x) + blo(kk[i + 2]) * bhi(sh.x) + blo(kk[i + 1]) * blo(sh.y) + blo(kk[i]) * bhi(sh.y);
    float s1 = bhi(kk[i + 3]) * blo(sh.z) + bhi(kk[i + 2]) * bhi(sh.z) + bhi(kk[i + 1]) * blo(sh.w) + bhi(kk[i]) * bhi(sh.w);
    a[i] = s0 * blo(vv[i]); b[i] = s1 * bhi(vv[i]);
  }
  u32x4 o0 = {pk2(a[7], a[6]), pk2(a[5], a[4]), pk2(a[3], a[2]), pk2(a[1], a[0])};
  u32x4 o1 = {pk2(b[7], b[6]), pk2(b[5], b[4]), pk2(b[3], b[2]), pk2(b[1], b[0])};
  u16* kvr = (u16*)(ws + WS_KVR);
  *(u32x4*)(kvr + (2 * cp) * S + (S - 8 - t0)) = o0;
  *(u32x4*)(kvr + (2 * cp + 1) * S + (S - 8 - t0)) = o1;
  u16* xt = (u16*)(ws + WS_XT);
  u32x4 x0 = {perm_lo(xx[0], xx[1]), perm_lo(xx[2], xx[3]), perm_lo(xx[4], xx[5]), perm_lo(xx[6], xx[7])};
  u32x4 x1 = {perm_hi(xx[0], xx[1]), perm_hi(xx[2], xx[3]), perm_hi(xx[4], xx[5]), perm_hi(xx[6], xx[7])};
  *(u32x4*)(xt + (2 * cp) * S + t0) = x0;
  *(u32x4*)(xt + (2 * cp + 1) * S + t0) = x1;
  float s0 = 0, s1 = 0;
#pragma unroll
  for (int i = 0; i < 4; ++i) { s0 += blo(r0[i]) + bhi(r0[i]); s1 += blo(r1[i]) + bhi(r1[i]); }
#pragma unroll
  for (int m = 1; m < 64; m <<= 1) { s0 += __shfl_xor(s0, m); s1 += __shfl_xor(s1, m); }
  const float m0 = s0 * (1.f / D), m1 = s1 * (1.f / D);
  float q0 = 0, q1 = 0;
#pragma unroll
  for (int i = 0; i < 4; ++i) {
    float d0 = blo(r0[i]) - m0, d1 = bhi(r0[i]) - m0, d2 = blo(r1[i]) - m1, d3 = bhi(r1[i]) - m1;
    q0 += d0 * d0 + d1 * d1; q1 += d2 * d2 + d3 * d3;
  }
#pragma unroll
  for (int m = 1; m < 64; m <<= 1) { q0 += __shfl_xor(q0, m); q1 += __shfl_xor(q1, m); }
  if (l == 0) {
    f32x4 st = {m0, rsqrtf(q0 * (1.f / D) + EPS), m1, rsqrtf(q1 * (1.f / D) + EPS)};
    *(f32x4*)(ws + WS_ST + (wg * 8 + w * 2) * 8) = st;
  }
}

constexpr int UR = 2304;
__global__ __launch_bounds__(256) void k_p2(const u16* __restrict__ hk, const u16* __restrict__ dvec, char* __restrict__ ws) {
  __shared__ __attribute__((aligned(16))) u16 ur[2 * UR];
  __shared__ __attribute__((aligned(16))) f32x4 red[4][4][64];
  const int wg = blockIdx.x;
  const int pair = (wg & 7) * 32 + (wg >> 3);
  const int tid = threadIdx.x, l = tid & 63, w = __builtin_amdgcn_readfirstlane(tid >> 6);
  const int cl = w >> 1, par = w & 1, ch = pair * 2 + cl;
  const int r = l & 15, q = l >> 4;
  const u16* kvr = (const u16*)(ws + WS_KVR) + pair * 2 * S;
  u32x4 s0 = ((const u32x4*)kvr)[tid], s1 = ((const u32x4*)(kvr + S))[tid];
  u32x4 F[32];
  const u16* hrow = hk + ch * S;
  const int fo = 32 * par - 15 + 8 * q + r;
#pragma unroll
  for (int a = 1; a < 32; ++a) F[a] = *(const u32x4_u*)(hrow + 64 * a + fo);
  {
    u32 e[8];
#pragma unroll
    for (int j = 0; j < 8; ++j) { int idx = fo + j; e[j] = hrow[idx < 0 ? 0 : idx]; if (idx < 0) e[j] = 0; }
    F[0] = (u32x4){e[0] | (e[1] << 16), e[2] | (e[3] << 16), e[4] | (e[5] << 16), e[6] | (e[7] << 16)};
  }
  u32x2 xv[4]; f32x4 st0[4], st1[4];
#pragma unroll
  for (int i = 0; i < 4; ++i) {
    const int t = 256 * (4 * par + i) + 16 * r + 4 * q;
    xv[i] = *(const u32x2*)((const u16*)(ws + WS_XT) + ch * S + t);
    st0[i] = *(const f32x4*)(ws + WS_ST + t * 8);
    st1[i] = *(const f32x4*)(ws + WS_ST + t * 8 + 16);
  }
  const float dd = blo(dvec[ch]);
  *(u32x4*)(ur + tid * 8) = s0;
  *(u32x4*)(ur + UR + tid * 8) = s1;
  if (tid < 64) *(u32x4*)(ur + (tid >> 5) * UR + S + (tid & 31) * 8) = (u32x4){0, 0, 0, 0};
  __syncthreads();
  f32x4 acc[8];
#pragma unroll
  for (int b = 0; b < 8; ++b) acc[b] = (f32x4){0, 0, 0, 0};
  const u16* gp = ur + cl * UR + 2224 + 32 * par - 16 * r + 8 * q;
#pragma unroll
  for (int n = 0; n < 32; ++n) {
    const u32x4 g = *(const u32x4*)(gp - 64 * n);
#pragma unroll
    for (int b = n >> 2; b < 8; ++b) acc[b] = mfma16(F[4 * b - n + 3], g, acc[b]);
  }
  f32x4 keep[4];
#pragma unroll
  for (int i = 0; i < 4; ++i) {
    f32x4 lo = acc[i], hi = acc[4 + i], snd;
#pragma unroll
    for (int e = 0; e < 4; ++e) { snd[e] = par ? lo[e] : hi[e]; keep[i][e] = par ? hi[e] : lo[e]; }
    red[w][i][l] = snd;
  }
  __syncthreads();
  u32x2 hv[4];
#pragma unroll
  for (int i = 0; i < 4; ++i) {
    f32x4 y = keep[i] + red[w ^ 1][i][l];
    const int t = 256 * (4 * par + i) + 16 * r + 4 * q;
    const u32x2 uu = *(const u32x2*)(ur + cl * UR + (2044 - t));
    const float z0 = y[0] + bhi(uu.y) * dd, z1 = y[1] + blo(uu.y) * dd, z2 = y[2] + bhi(uu.x) * dd, z3 = y[3] + blo(uu.x) * dd;
    const float x0 = blo(xv[i].x), x1 = bhi(xv[i].x), x2 = blo(xv[i].y), x3 = bhi(xv[i].y);
    const float h0 = x0 + (x0 - st0[i][0]) * st0[i][1] * z0, h1 = x1 + (x1 - st0[i][2]) * st0[i][3] * z1;
    const float h2 = x2 + (x2 - st1[i][0]) * st1[i][1] * z2, h3 = x3 + (x3 - st1[i][2]) * st1[i][3] * z3;
    hv[i] = (u32x2){pk2(h0, h1), pk2(h2, h3)};
  }
  const int fsw = ((ch >> 1) & 1) | (((ch >> 3) & 1) << 1);
#pragma unroll
  for (int i = 0; i < 4; i += 2) {
    auto sx = __builtin_amdgcn_permlane16_swap(hv[i].x, hv[i + 1].x, false, false);
    auto sy = __builtin_amdgcn_permlane16_swap(hv[i].y, hv[i + 1].y, false, false);
    u32x4 ov = {sx[0], sy[0], sx[1], sy[1]};
    const int b = 4 * par + i + (q & 1);
    const int tile = 4 * b + (r >> 2), mt = r & 3;
    char* dst = ws + WS_HT + (size_t)tile * (D * 128) + ch * 128 + ((mt ^ fsw) * 32) + (q >> 1) * 16;
    *(u32x4*)dst = ov;
  }
}

__device__ __forceinline__ u32x4 tr_frag(const char* lds, int off) {
  s16x4 a = __builtin_amdgcn_ds_read_tr16_b64_v4i16((LAS s16x4*)(lds + off));
  s16x4 b = __builtin_amdgcn_ds_read_tr16_b64_v4i16((LAS s16x4*)(lds + off + 512));
  u32x2 x = __builtin_bit_cast(u32x2, a), y = __builtin_bit_cast(u32x2, b);
  return (u32x4){x.x, x.y, y.x, y.y};
}

__global__ __launch_bounds__(256, 1) void k_p3(const u16* __restrict__ w1, char* __restrict__ ws) {
  __shared__ __attribute__((aligned(16))) char hs[65536 + 512];
  const int wg = blockIdx.x, tile = (wg & 7) * 4 + ((wg >> 3) & 3), j = wg >> 5;
  const int tid = threadIdx.x, l = tid & 63, w = __builtin_amdgcn_readfirstlane(tid >> 6);
  const int r = l & 15, q = l >> 4;
  const char* src = ws + WS_HT + (size_t)tile * 65536;
#pragma unroll
  for (int i = 0; i < 16; ++i)
    __builtin_amdgcn_global_load_lds((const u32*)(src + (i * 256 + tid) * 16), (LAS u32*)(hs + (i * 256 + w * 64) * 16), 16, 0, 0);
  f32x2* stat = (f32x2*)(hs + 65536);
  const int row0 = 8 * q + (r >> 2);
  const int fs = ((row0 >> 1) & 1) | (((row0 >> 3) & 1) << 1);
  int toff[4];
#pragma unroll
  for (int mt = 0; mt < 4; ++mt) toff[mt] = row0 * 128 + ((mt ^ fs) * 32) + 8 * (r & 3);
  const u32x4 ones = {0x3f803f80u, 0x3f803f80u, 0x3f803f80u, 0x3f803f80u};
  f32x4 acc[4][4], c1[4];
#pragma unroll
  for (int i = 0; i < 4; ++i) { c1[i] = (f32x4){0, 0, 0, 0};
#pragma unroll
    for (int mt = 0; mt < 4; ++mt) acc[i][mt] = (f32x4){0, 0, 0, 0}; }
  char* gdst = ws + WS_G + ((size_t)(tile * 8 + j) * 7) * 4096 + l * 16;
  if (w < 3) {
    const u16* wp = w1 + (8 * q) * FF + 224 * j + 64 * w + 4 * r;
    u32x2 raw[16][8];
#pragma unroll
    for (int ks = 0; ks < 16; ++ks)
#pragma unroll
      for (int jj = 0; jj < 8; ++jj) raw[ks][jj] = *(const u32x2*)(wp + (32 * ks + jj) * FF);
    __syncthreads();
#pragma unroll
    for (int ks = 0; ks < 16; ++ks) {
      u32x4 f[4];
      f[0] = (u32x4){perm_lo(raw[ks][0].x, raw[ks][1].x), perm_lo(raw[ks][2].x, raw[ks][3].x), perm_lo(raw[ks][4].x, raw[ks][5].x), perm_lo(raw[ks][6].x, raw[ks][7].x)};
      f[1] = (u32x4){perm_hi(raw[ks][0].x, raw[ks][1].x), perm_hi(raw[ks][2].x, raw[ks][3].x), perm_hi(raw[ks][4].x, raw[ks][5].x), perm_hi(raw[ks][6].x, raw[ks][7].x)};
      f[2] = (u32x4){perm_lo(raw[ks][0].y, raw[ks][1].y), perm_lo(raw[ks][2].y, raw[ks][3].y), perm_lo(raw[ks][4].y, raw[ks][5].y), perm_lo(raw[ks][6].y, raw[ks][7].y)};
      f[3] = (u32x4){perm_hi(raw[ks][0].y, raw[ks][1].y), perm_hi(raw[ks][2].y, raw[ks][3].y), perm_hi(raw[ks][4].y, raw[ks][5].y), perm_hi(raw[ks][6].y, raw[ks][7].y)};
#pragma unroll
      for (int mt = 0; mt < 4; ++mt) {
        const u32x4 hf = tr_frag(hs, toff[mt] + ks * 4096);
#pragma unroll
        for (int i = 0; i < 4; ++i) acc[i][mt] = mfma16(f[i], hf, acc[i][mt]);
      }
#pragma unroll
      for (int i = 0; i < 4; ++i) c1[i] = mfma16(f[i], ones, c1[i]);
    }
    __syncthreads();
#pragma unroll
    for (int mt = 0; mt < 4; ++mt) {
      const f32x2 st = stat[16 * mt + r];
      float g[16];
#pragma unroll
      for (int ii = 0; ii < 4; ++ii)
#pragma unroll
        for (int i = 0; i < 4; ++i) g[4 * ii + i] = fmaxf(acc[i][mt][ii] - st.x * c1[i][ii], 0.f) * st.y;
      u32x4 g0 = {pk2(g[0], g[1]), pk2(g[2], g[3]), pk2(g[4], g[5]), pk2(g[6], g[7])};
      u32x4 g1 = {pk2(g[8], g[9]), pk2(g[10], g[11]), pk2(g[12], g[13]), pk2(g[14], g[15])};
      *(u32x4*)(gdst + ((2 * w) * 4 + mt) * 1024) = g0;
      *(u32x4*)(gdst + ((2 * w + 1) * 4 + mt) * 1024) = g1;
    }
  } else {
    const u16* wp = w1 + (8 * q) * FF + 224 * j + 192 + 2 * r;
    u32 raw[16][8];
#pragma unroll
    for (int ks = 0; ks < 16; ++ks)
#pragma unroll
      for (int jj = 0; jj < 8; ++jj) raw[ks][jj] = *(const u32*)(wp + (32 * ks + jj) * FF);
    __syncthreads();
#pragma unroll
    for (int ks = 0; ks < 16; ++ks) {
      u32x4 f[2];
      f[0] = (u32x4){perm_lo(raw[ks][0], raw[ks][1]), perm_lo(raw[ks][2], raw[ks][3]), perm_lo(raw[ks][4], raw[ks][5]), perm_lo(raw[ks][6], raw[ks][7])};
      f[1] = (u32x4){perm_hi(raw[ks][0], raw[ks][1]), perm_hi(raw[ks][2], raw[ks][3]), perm_hi(raw[ks][4], raw[ks][5]), perm_hi(raw[ks][6], raw[ks][7])};
#pragma unroll
      for (int mt = 0; mt < 4; ++mt) {
        const u32x4 hf = tr_frag(hs, toff[mt] + ks * 4096);
        acc[0][mt] = mfma16(f[0], hf, acc[0][mt]);
        acc[1][mt] = mfma16(f[1], hf, acc[1][mt]);
        acc[2][mt] = mfma16(ones, hf, acc[2][mt]);
        acc[3][mt] = mfma16(hf, hf, acc[3][mt]);
      }
      c1[0] = mfma16(f[0], ones, c1[0]);
      c1[1] = mfma16(f[1], ones, c1[1]);
    }
    const int di = r & 3;
#pragma unroll
    for (int mt = 0; mt < 4; ++mt) {
      const float sq = di == 0 ? acc[3][mt][0] : di == 1 ? acc[3][mt][1] : di == 2 ? acc[3][mt][2] : acc[3][mt][3];
      const float mean = acc[2][mt][0] * (1.f / D);
      const float var = fmaxf(sq * (1.f / D) - mean * mean, 0.f);
      if ((r >> 2) == q) stat[16 * mt + r] = (f32x2){mean, rsqrtf(var + EPS)};
    }
    __syncthreads();
#pragma unroll
    for (int mt = 0; mt < 4; ++mt) {
      const f32x2 st = stat[16 * mt + r];
      float g[8];
#pragma unroll
      for (int ii = 0; ii < 4; ++ii)
#pragma unroll
        for (int i = 0; i < 2; ++i) g[2 * ii + i] = fmaxf(acc[i][mt][ii] - st.x * c1[i][ii], 0.f) * st.y;
      u32x4 g0 = {pk2(g[0], g[1]), pk2(g[2], g[3]), pk2(g[4], g[5]), pk2(g[6], g[7])};
      *(u32x4*)(gdst + (6 * 4 + mt) * 1024) = g0;
    }
  }
}

__global__ __launch_bounds__(256, 1) void k_p4(const u16* __restrict__ w2, const char* __restrict__ ws, u16* __restrict__ out) {
  __shared__ __attribute__((aligned(16))) f32x4 red[4][4][4][64];
  const int wg = blockIdx.x, tile = (wg & 7) * 4 + ((wg >> 3) & 3), j = wg >> 5;
  const int tid = threadIdx.x, l = tid & 63, w = __builtin_amdgcn_readfirstlane(tid >> 6);
  const int r = l & 15, q = l >> 4;
  const char* gsrc = ws + WS_G + ((size_t)(tile * 8 + 2 * w) * 7) * 4096 + l * 16;
  const u16* wp = w2 + (size_t)(448 * w) * D + 64 * j + 4 * r;
  f32x4 acc[4][4];
#pragma unroll
  for (int mt = 0; mt < 4; ++mt)
#pragma unroll
    for (int i = 0; i < 4; ++i) acc[mt][i] = (f32x4){0, 0, 0, 0};
  u32x2 hres[4];
#pragma unroll
  for (int i = 0; i < 4; ++i) {
    const int ch = 64 * j + 4 * r + i;
    const int fsw = ((ch >> 1) & 1) | (((ch >> 3) & 1) << 1);
    hres[i] = *(const u32x2*)(ws + WS_HT + (size_t)tile * 65536 + ch * 128 + ((w ^ fsw) * 32) + q * 8);
  }
#pragma unroll
  for (int s = 0; s < 14; ++s) {
    const int sl = s / 7, k7 = s % 7;
    const int frow = 224 * sl + (k7 < 6 ? 64 * (k7 >> 1) + 8 * (k7 & 1) : 192);
    const int qs = k7 < 6 ? 16 : 8;
    u32x2 raw[8];
#pragma unroll
    for (int jj = 0; jj < 8; ++jj) raw[jj] = *(const u32x2*)(wp + (frow + qs * q + jj) * D);
    u32x4 g[4];
#pragma unroll
    for (int mt = 0; mt < 4; ++mt) g[mt] = *(const u32x4*)(gsrc + (s * 4 + mt) * 1024);
    u32x4 f[4];
    f[0] = (u32x4){perm_lo(raw[0].x, raw[1].x), perm_lo(raw[2].x, raw[3].x), perm_lo(raw[4].x, raw[5].x), perm_lo(raw[6].x, raw[7].x)};
    f[1] = (u32x4){perm_hi(raw[0].x, raw[1].x), perm_hi(raw[2].x, raw[3].x), perm_hi(raw[4].x, raw[5].x), perm_hi(raw[6].x, raw[7].x)};
    f[2] = (u32x4){perm_lo(raw[0].y, raw[1].y), perm_lo(raw[2].y, raw[3].y), perm_lo(raw[4].y, raw[5].y), perm_lo(raw[6].y, raw[7].y)};
    f[3] = (u32x4){perm_hi(raw[0].y, raw[1].y), perm_hi(raw[2].y, raw[3].y), perm_hi(raw[4].y, raw[5].y), perm_hi(raw[6].y, raw[7].y)};
#pragma unroll
    for (int mt = 0; mt < 4; ++mt)
#pragma unroll
      for (int i = 0; i < 4; ++i) acc[mt][i] = mfma16(g[mt], f[i], acc[mt][i]);
  }
#pragma unroll
  for (int mt = 0; mt < 4; ++mt)
#pragma unroll
    for (int i = 0; i < 4; ++i) red[w][mt][i][l] = acc[mt][i];
  __syncthreads();
  f32x4 o[4];
#pragma unroll
  for (int i = 0; i < 4; ++i) o[i] = (red[0][w][i][l] + red[1][w][i][l]) + (red[2][w][i][l] + red[3][w][i][l]);
#pragma unroll
  for (int ii = 0; ii < 4; ++ii) {
    float hv[4];
#pragma unroll
    for (int i = 0; i < 4; ++i) { const u32 p = (ii & 2) ? hres[i].y : hres[i].x; hv[i] = (ii & 1) ? bhi(p) : blo(p); }
    u32x2 ov = {pk2(o[0][ii] + hv[0], o[1][ii] + hv[1]), pk2(o[2][ii] + hv[2], o[3][ii] + hv[3])};
    *(u32x2*)(out + (size_t)(tile * 64 + 16 * w + 4 * q + ii) * D + 64 * j + 4 * r) = ov;
  }
}


extern "C" void kernel_launch(void* const* d_in, const int* in_sizes, int n_in, void* d_out, int out_size, void* d_ws,
                              size_t ws_size, hipStream_t stream) {
  char* ws = (char*)d_ws;
  k_p1<<<256, 256, 0, stream>>>((const u32*)d_in[0], (const u32*)d_in[1], (const u32*)d_in[2], (const u32x4*)d_in[3], ws);
  k_p2<<<256, 256, 0, stream>>>((const u16*)d_in[4], (const u16*)d_in[5], ws);
  k_p3<<<256, 256, 0, stream>>>((const u16*)d_in[7], ws);
  k_p4<<<256, 256, 0, stream>>>((const u16*)d_in[8], ws, (u16*)d_out);

}
```

```cpp
#include <hip/hip_runtime.h>
#include <hip/hip_bf16.h>
#include <stdint.h>

typedef unsigned short u16;
typedef unsigned int u32;
typedef unsigned long long u64;
typedef __bf16 bf16x8 __attribute__((ext_vector_type(8)));
typedef __bf16 bf16x2 __attribute__((ext_vector_type(2)));
typedef short s16x4 __attribute__((ext_vector_type(4)));
typedef short s16x2 __attribute__((ext_vector_type(2)));
typedef float f32x4 __attribute__((ext_vector_type(4)));
typedef float f32x2 __attribute__((ext_vector_type(2)));
typedef u32 u32x4 __attribute__((ext_vector_type(4)));
typedef u32 u32x2 __attribute__((ext_vector_type(2)));
typedef __amdgpu_buffer_rsrc_t rsrc_t;
#define LAS __attribute__((address_space(3)))
#define GAS __attribute__((address_space(1)))

constexpr int S = 2048, D = 512, FF = 1792;
constexpr float EPS = 1e-5f;
constexpr u32 POISON = 0xffffffffu;
constexpr u32 TAGX = 0x5bd1e995u;
constexpr int SPIN_CAP = 1 << 20;

constexpr u32 WS_EPOCH = 0;
constexpr u32 WS_FLAGA = 4096;
constexpr u32 WS_FLAGC = WS_FLAGA + 256 * 128;
constexpr u32 WS_STATS = WS_FLAGC + 256 * 128;
constexpr u32 WS_KVR = 1u << 17;
constexpr u32 WS_XT = WS_KVR + D * S * 2;
constexpr u32 WS_HT = WS_XT + D * S * 2;
constexpr u32 WS_W1T = WS_HT + D * S * 2;
constexpr u32 WS_W2T = WS_W1T + D * FF * 2;
constexpr u32 WS_G = WS_W2T + D * FF * 2;
constexpr u32 WS_END = WS_G + S * FF * 2;
constexpr u32 WS_STAMP = 32u << 20;

constexpr int L_E = 0;
constexpr int E_CP = 4160, E_CH = 8 * E_CP;
constexpr int L_HS = 0, L_ST2 = 65536;
constexpr int L_UR = 2 * E_CH;
constexpr int UR_CH = 4608;
constexpr int L_RED = L_UR + 2 * UR_CH;
constexpr int L_K = L_RED, L_V = L_K + 9 * 1040, L_X = L_V + 8 * 1040, L_W = L_X + 8 * 1040;
constexpr int W_ROW = 272;
constexpr int L_ST1 = L_RED + 65536;
constexpr int L_END = L_ST1 + 16384;

__device__ __forceinline__ float blo(u32 v) { return __uint_as_float(v << 16); }
__device__ __forceinline__ float bhi(u32 v) { return __uint_as_float(v & 0xffff0000u); }
__device__ __forceinline__ u32 pk2(float lo, float hi) { bf16x2 r = {(__bf16)lo, (__bf16)hi}; return __builtin_bit_cast(u32, r); }
__device__ __forceinline__ u32 nopoison(u32 v) { return v < 0xfffffffeu ? v : 0xfffffffeu; }
__device__ __forceinline__ u32x4 nopoison4(u32x4 v) { return (u32x4){nopoison(v.x), nopoison(v.y), nopoison(v.z), nopoison(v.w)}; }
__device__ __forceinline__ u32 relu2(u32 v) { return __builtin_bit_cast(u32, __builtin_elementwise_max(__builtin_bit_cast(s16x2, v), (s16x2){0, 0})); }
__device__ __forceinline__ u32 umax(u32 a, u32 b) { return a > b ? a : b; }
__device__ __forceinline__ u32 max4(u32x4 v, u32 m) { return umax(umax(umax(v.x, v.y), m), umax(v.z, v.w)); }
__device__ __forceinline__ u32 or16(const u32x4 (&g)[4]) { const u32x4 t = (g[0] | g[1]) | (g[2] | g[3]); return (t.x | t.y) | (t.z | t.w); }
__device__ __forceinline__ f32x4 mfma16(u32x4 a, u32x4 b, f32x4 c) {
  return __builtin_amdgcn_mfma_f32_16x16x32_bf16(__builtin_bit_cast(bf16x8, a), __builtin_bit_cast(bf16x8, b), c, 0, 0, 0);
}
__device__ __forceinline__ u32 perm_lo(u32 a, u32 b) { return __builtin_amdgcn_perm(b, a, 0x05040100u); }
__device__ __forceinline__ u32 perm_hi(u32 a, u32 b) { return __builtin_amdgcn_perm(b, a, 0x07060302u); }
__device__ __forceinline__ rsrc_t mkr(const void* p, u32 bytes) { return __builtin_amdgcn_make_buffer_rsrc((void*)p, 0, bytes, 0x00020000); }
constexpr int NT = 2, SC1 = 16;
template <int AUX> __device__ __forceinline__ u32x4 ld16(rsrc_t r, u32 off, u32 soff = 0) { return __builtin_amdgcn_raw_buffer_load_b128(r, off, soff, AUX); }
template <int AUX> __device__ __forceinline__ u32x2 ld8(rsrc_t r, u32 off) { return __builtin_amdgcn_raw_buffer_load_b64(r, off, 0, AUX); }
template <int AUX> __device__ __forceinline__ void st16(rsrc_t r, u32 off, u32x4 v) { __builtin_amdgcn_raw_buffer_store_b128(v, r, off, 0, AUX); }
template <int AUX> __device__ __forceinline__ void st8(rsrc_t r, u32 off, u32x2 v) { __builtin_amdgcn_raw_buffer_store_b64(v, r, off, 0, AUX); }
__device__ __forceinline__ u32x2 tr8(const char* lds, int off) {
  return __builtin_bit_cast(u32x2, __builtin_amdgcn_ds_read_tr16_b64_v4i16((LAS s16x4*)(lds + off)));
}
__device__ __forceinline__ u32x4 tr16(const char* lds, int off0, int off1) { u32x2 a = tr8(lds, off0), b = tr8(lds, off1); return (u32x4){a.x, a.y, b.x, b.y}; }
template <int CTRL> __device__ __forceinline__ float dpp(float v) { return __uint_as_float(__builtin_amdgcn_update_dpp(0u, __float_as_uint(v), CTRL, 0xf, 0xf, true)); }
__device__ __forceinline__ float wave_sum(float v) {
  v += dpp<0xb1>(v); v += dpp<0x4e>(v); v += dpp<0x141>(v); v += dpp<0x140>(v);
  auto a = __builtin_amdgcn_permlane16_swap(__float_as_uint(v), __float_as_uint(v), false, false);
  v = __uint_as_float(a[0]) + __uint_as_float(a[1]);
  auto b = __builtin_amdgcn_permlane32_swap(__float_as_uint(v), __float_as_uint(v), false, false);
  return __uint_as_float(b[0]) + __uint_as_float(b[1]);
}
#define FENCE() asm volatile("" ::: "memory")
#define DRAIN() asm volatile("s_waitcnt vmcnt(0)" ::: "memory")
#define SPIN _Pragma("nounroll") for (int spin = 0; spin < SPIN_CAP; ++spin)
#define SCHED() __builtin_amdgcn_sched_barrier(0)
#define BAR() do { asm volatile("s_waitcnt lgkmcnt(0)" ::: "memory"); __builtin_amdgcn_s_barrier(); FENCE(); } while (0)
#define STAMP(i) do {} while (0)

#define CODE_KB 19

__global__ __launch_bounds__(512, 2) void h3_fused(const u16* __restrict__ px, const u16* __restrict__ pk, const u16* __restrict__ pv,
                                                   const u16* __restrict__ pshift, const u16* __restrict__ phk, const u16* __restrict__ pdv,
                                                   const u16* __restrict__ pw1, const u16* __restrict__ pw2, u16* __restrict__ pout, char* pws) {
  __shared__ __attribute__((aligned(16))) char lds[L_END];
  STAMP(0);
  const int wg = blockIdx.x, x = wg & 7, y = wg >> 3;
  const int tid = threadIdx.x, l = tid & 63, w = __builtin_amdgcn_readfirstlane(tid >> 6);
  const int r = l & 15, q = l >> 4;
  u32 codepf = 0;
  if (tid < CODE_KB * 8) { u64 pc; asm volatile("s_getpc_b64 %0" : "=s"(pc)); codepf = *(const GAS u32*)((pc & ~255ull) + tid * 128); }
  const rsrc_t rx = mkr(px, S * D * 2), rk = mkr(pk, S * D * 2), rv = mkr(pv, S * D * 2), rws = mkr(pws, WS_END);
  const int pair = 32 * x + y, tile = 4 * x + (y & 3), j = y >> 2;

  const u32 toff = (64 * y + (tid >> 3)) * 1024 + 128 * x + (tid & 7) * 16;
  const u32x4 kreg = ld16<NT>(rk, toff), vreg = ld16<NT>(rv, toff);
  u32x4 halo = {0, 0, 0, 0};
  if (w == 0) halo = ld16<NT>(rk, toff - 8192);
  const int o8 = l & 7, pl = 8 * (w & 3) + (l >> 3);
  const u32x4 sh = ld16<0>(mkr(pshift, D * 8), (32 * x + pl) * 16);
  FENCE();
  const u32 p1off = (w < 4 ? WS_KVR + 4080 - 128 * y - 16 * o8 : WS_XT + 128 * y + 16 * o8) + (64 * x + 2 * pl) * 4096;
  const u32x4 pois = {POISON, POISON, POISON, POISON};
  st16<SC1>(rws, p1off, pois); st16<SC1>(rws, p1off + 4096, pois);
  const int cch = tid >> 8, oct = tid & 255, chg = 2 * pair + cch;
  const u32 hoff = WS_HT + (oct >> 3) * 65536 + chg * 128 + ((((oct >> 1) & 3) ^ ((pair & 1) | ((pair >> 1) & 2))) * 32) + (oct & 1) * 16;
  st16<SC1>(rws, hoff, pois);
  FENCE();
  const u32 epoch = __builtin_amdgcn_raw_buffer_load_b32(rws, WS_EPOCH, 0, SC1);
  FENCE();
  const rsrc_t rh = mkr(phk, D * S * 2);
  const u32x4 fcur = ld16<0>(rh, chg * 4096 + oct * 16);
  u32x4 fprev = ld16<0>(rh, chg * 4096 + oct * 16 - 16);
  const u32x4 xreg = ld16<NT>(rx, toff);
  const u32x4 srow = ld16<NT>(rx, (8 * wg + w) * 1024 + l * 16);
  u32x4 wraw[8];
  const bool retile = (w == 4 || w == 5) && wg < 224;
  if (retile) {
    const rsrc_t rw = mkr(w == 4 ? pw1 : pw2, D * FF * 2);
    const u32 rs = w == 4 ? FF * 2 : D * 2;
    const u32 b = w == 4 ? (wg & 15) * 32 * (FF * 2) + (wg >> 4) * 256 : (wg >> 2) * 32 * (D * 2) + (wg & 3) * 256;
#pragma unroll
    for (int i = 0; i < 8; ++i) wraw[i] = ld16<NT>(rw, b + (4 * i + q) * rs + r * 16);
  }
  FENCE();
  *(u32x4*)(lds + L_K + (w + 1) * 1040 + l * 16) = kreg;
  *(u32x4*)(lds + L_V + w * 1040 + l * 16) = vreg;
  if (w == 0) *(u32x4*)(lds + L_K + l * 16) = halo;
  STAMP(1);
  BAR();
  if (w < 4) {
    const char* kb = lds + L_K + (o8 + 1) * 1040 + pl * 4;
    const char* vb = lds + L_V + o8 * 1040 + pl * 4;
    u32 kk[11];
#pragma unroll
    for (int i = 0; i < 11; ++i) kk[i] = *(const u32*)(kb + (i < 3 ? (5 + i) * 128 - 1040 : (i - 3) * 128));
    const f32x2 s0 = {blo(sh.x), blo(sh.z)}, s1 = {bhi(sh.x), bhi(sh.z)}, s2 = {blo(sh.y), blo(sh.w)}, s3 = {bhi(sh.y), bhi(sh.w)};
    f32x2 kf[11], a[8];
#pragma unroll
    for (int i = 0; i < 11; ++i) kf[i] = (f32x2){blo(kk[i]), bhi(kk[i])};
#pragma unroll
    for (int i = 0; i < 8; ++i) {
      const u32 vv = *(const u32*)(vb + i * 128);
      a[i] = (kf[i + 3] * s0 + kf[i + 2] * s1 + kf[i + 1] * s2 + kf[i] * s3) * (f32x2){blo(vv), bhi(vv)};
    }
    st16<SC1>(rws, p1off, nopoison4((u32x4){pk2(a[7].x, a[6].x), pk2(a[5].x, a[4].x), pk2(a[3].x, a[2].x), pk2(a[1].x, a[0].x)}));
    st16<SC1>(rws, p1off + 4096, nopoison4((u32x4){pk2(a[7].y, a[6].y), pk2(a[5].y, a[4].y), pk2(a[3].y, a[2].y), pk2(a[1].y, a[0].y)}));
  }
  FENCE();
  STAMP(2);
  const u32 tag = __builtin_amdgcn_readfirstlane(epoch) + 1u;
  const u32x2 tagv = {tag, tag ^ TAGX};
  if (l == 0) st8<SC1>(rws, WS_FLAGA + wg * 128 + w * 8, tagv);
  FENCE();
  *(u32x4*)(lds + L_X + w * 1040 + l * 16) = xreg;
  {
    if (oct == 0) fprev = (u32x4){0, 0, 0, 0};
    const u32 d[8] = {fprev.x, fprev.y, fprev.z, fprev.w, fcur.x, fcur.y, fcur.z, fcur.w};
    u32 e[7];
#pragma unroll
    for (int i = 0; i < 7; ++i) e[i] = __builtin_amdgcn_alignbyte(d[i + 1], d[i], 2);
    char* eb = lds + L_E + cch * E_CH + (oct + 1) * 16;
#pragma unroll
    for (int s = 0; s < 4; ++s) {
      *(u32x4*)(eb + (2 * s) * E_CP) = (u32x4){e[s], e[s + 1], e[s + 2], e[s + 3]};
      *(u32x4*)(eb + (2 * s + 1) * E_CP) = (u32x4){d[s + 1], d[s + 2], d[s + 3], d[s + 4]};
    }
    if (tid < 16) *(u32x4*)(lds + L_E + tid * E_CP) = (u32x4){0, 0, 0, 0};
    if (tid >= 64 && tid < 128) *(u32x4*)(lds + L_UR + (l >> 5) * UR_CH + 4096 + (l & 31) * 16) = (u32x4){0, 0, 0, 0};
  }
  BAR();
  if (w >= 4) {
    const char* xb = lds + L_X + o8 * 1040 + pl * 4;
    u32 xx[8];
#pragma unroll
    for (int i = 0; i < 8; ++i) xx[i] = *(const u32*)(xb + i * 128);
    st16<SC1>(rws, p1off, nopoison4((u32x4){perm_lo(xx[0], xx[1]), perm_lo(xx[2], xx[3]), perm_lo(xx[4], xx[5]), perm_lo(xx[6], xx[7])}));
    st16<SC1>(rws, p1off + 4096, nopoison4((u32x4){perm_hi(xx[0], xx[1]), perm_hi(xx[2], xx[3]), perm_hi(xx[4], xx[5]), perm_hi(xx[6], xx[7])}));
  }
  {
    float v[8];
#pragma unroll
    for (int i = 0; i < 4; ++i) { v[2 * i] = blo(srow[i]); v[2 * i + 1] = bhi(srow[i]); }
    const float m = wave_sum(((v[0] + v[1]) + (v[2] + v[3])) + ((v[4] + v[5]) + (v[6] + v[7]))) * (1.f / D);
    float qq = 0;
#pragma unroll
    for (int i = 0; i < 8; ++i) qq += (v[i] - m) * (v[i] - m);
    const float rs = rsqrtf(wave_sum(qq) * (1.f / D) + EPS);
    if (l == 0) st16<SC1>(rws, WS_STATS + (8 * wg + w) * 16, (u32x4){__float_as_uint(m), tagv.x, __float_as_uint(rs), tagv.y});
  }
  if (retile) {
    char* wb = lds + L_W + (w - 4) * (32 * W_ROW);
#pragma unroll
    for (int i = 0; i < 8; ++i) *(u32x4*)(wb + (4 * i + q) * W_ROW + r * 16) = wraw[i];
    const int ro = (w == 4 ? 8 * q : 4 * q) + (r >> 2), rt = w == 4 ? 4 : 16;
    const int a0 = ro * W_ROW + (r & 3) * 8, a1 = a0 + rt * W_ROW;
    const u32 db = (w == 4 ? WS_W1T + ((wg >> 4) * 4 * 16 + (wg & 15)) * 2048 : WS_W2T + ((wg & 3) * 2 * 56 + (wg >> 2)) * 4096) + l * 16;
#pragma unroll
    for (int i = 0; i < 8; ++i) {
      const u32 di = w == 4 ? (i >> 1) * 32768 + (i & 1) * 1024 : (i >> 2) * (56 * 4096) + (i & 3) * 1024;
      st16<SC1>(rws, db + di, tr16(wb, a0 + i * 32, a1 + i * 32));
    }
  }
  asm volatile("" :: "v"(codepf));
  STAMP(3);
  DRAIN();
  if (retile && w == 4 && l == 0) st8<SC1>(rws, WS_FLAGC + wg * 128, tagv);
  STAMP(4);
  SPIN {
    FENCE();
    const u32x2 fa = ld8<SC1>(rws, WS_FLAGA + (x + 8 * (31 - (oct >> 3))) * 128 + (y >> 3) * 8);
    const u32x2 fb = ld8<SC1>(rws, WS_FLAGA + (x + 8 * (oct >> 3)) * 128 + 32 + (y >> 3) * 8);
    if (__all((((fa.x ^ tagv.x) | (fa.y ^ tagv.y)) | ((fb.x ^ tagv.x) | (fb.y ^ tagv.y))) == 0)) break;
  }
  {
    u32x4 u;
    SPIN {
      FENCE();
      u = ld16<SC1>(rws, WS_KVR + chg * 4096 + oct * 16);
      if (__all(max4(u, 0) != POISON)) break;
    }
    *(u32x4*)(lds + L_UR + cch * UR_CH + oct * 16) = u;
  }
  STAMP(5);
  const u32 goff = WS_G + (tile * 56 + 7 * j + w) * 4096 + l * 16;
  if (w < 7) {
#pragma unroll
    for (int mt = 0; mt < 4; ++mt) st16<SC1>(rws, goff + mt * 1024, pois);
  }
  FENCE();
  u32x4 xt = ld16<SC1>(rws, WS_XT + chg * 4096 + oct * 16);
  u32x4 stv[4];
#pragma unroll
  for (int i = 0; i < 4; ++i) stv[i] = ld16<SC1>(rws, WS_STATS + (i * 512 + tid) * 16);
  const u32 fcoff = WS_FLAGC + (((7 * j + (w < 7 ? w : 0)) >> 2) * 16 + r) * 128;
  u32x2 fc = ld8<SC1>(rws, fcoff);
  const float dd = blo(pdv[chg]);
  BAR();
  f32x4 acc[8];
#pragma unroll
  for (int b = 0; b < 8; ++b) acc[b] = (f32x4){0, 0, 0, 0};
  u32x4 w1f[32];
  {
    const char* fb = lds + L_E + (w >> 2) * E_CH + (r & 7) * E_CP + (4 * (w & 3) + q + (r >> 3)) * 16;
    const char* gb = lds + L_UR + (w >> 2) * UR_CH + (2032 - 1792 + 32 * (w & 3) - 16 * r + 8 * q) * 2;
#pragma unroll
    for (int it = 0; it < 2; ++it) {
      u32x4 F[8], G[8];
#pragma unroll
      for (int i = 0; i < 8; ++i) { F[i] = *(const u32x4*)(fb + it * 256 + i * 512); G[i] = *(const u32x4*)(gb + it * 256 + (7 - i) * 512); }
#pragma unroll
      for (int b = 0; b < 8; ++b) {
#pragma unroll
        for (int n = 0; n <= b; ++n) acc[b] = mfma16(F[b - n], G[n], acc[b]);
        if (it == 1) *(f32x4*)(lds + L_RED + (w * 8 + b) * 1024 + (l ^ ((l >> 5) << 3)) * 16) = acc[b];
      }
      if (it == 0) {
        SPIN {
          u32 bad = (fc.x ^ tagv.x) | (fc.y ^ tagv.y);
#pragma unroll
          for (int i = 0; i < 4; ++i) bad |= (stv[i].y ^ tagv.x) | (stv[i].w ^ tagv.y);
          if (__all(bad == 0 && max4(xt, 0) != POISON)) break;
          FENCE();
          xt = ld16<SC1>(rws, WS_XT + chg * 4096 + oct * 16);
#pragma unroll
          for (int i = 0; i < 4; ++i) stv[i] = ld16<SC1>(rws, WS_STATS + (i * 512 + tid) * 16);
          fc = ld8<SC1>(rws, fcoff);
        }
        FENCE();
#pragma unroll
        for (int i = 0; i < 4; ++i) {
          const int t = i * 512 + tid;
          *(u32x2*)(lds + L_ST1 + ((t >> 1) & 3) * 4096 + (t >> 3) * 16 + (t & 1) * 8) = (u32x2){stv[i].x, stv[i].z};
        }
        STAMP(6);
        const u32 w1b = w < 7 ? WS_W1T + (7 * j + w) * 32768 : 0x40000000u;
#pragma unroll
        for (int i = 0; i < 32; ++i) w1f[i] = ld16<SC1>(rws, l * 16 + (i & 3) * 1024, w1b + (i >> 2) * 4096);
        SCHED();
      }
    }
  }
  STAMP(7);
  BAR();
  {
    const int hh = oct & 1, sl = (32 * hh + ((oct >> 1) & 15)) ^ (hh << 3);
    const char* rb = lds + L_RED + (cch * 32 + (oct >> 5)) * 1024 + sl * 16;
    f32x4 y0 = *(const f32x4*)(rb), y1 = *(const f32x4*)(rb + 256);
#pragma unroll
    for (int c = 1; c < 4; ++c) { y0 += *(const f32x4*)(rb + c * 8192); y1 += *(const f32x4*)(rb + c * 8192 + 256); }
    const u32x4 u = *(const u32x4*)(lds + L_UR + cch * UR_CH + (255 - oct) * 16);
    const float uu[8] = {bhi(u.w), blo(u.w), bhi(u.z), blo(u.z), bhi(u.y), blo(u.y), bhi(u.x), blo(u.x)};
    u32x4 hv;
#pragma unroll
    for (int i = 0; i < 4; ++i) {
      const f32x4 st = *(const f32x4*)(lds + L_ST1 + i * 4096 + oct * 16);
      const float x0 = blo(xt[i]), x1 = bhi(xt[i]);
      const float z0 = (i < 2 ? y0[2 * i] : y1[2 * i - 4]) + uu[2 * i] * dd, z1 = (i < 2 ? y0[2 * i + 1] : y1[2 * i - 3]) + uu[2 * i + 1] * dd;
      hv[i] = nopoison(pk2(x0 + (x0 - st[0]) * st[1] * z0, x1 + (x1 - st[2]) * st[3] * z1));
    }
    st16<SC1>(rws, hoff, hv);
  }
  SCHED();
  STAMP(8);
  const u32x4 ones = {0x3f803f80u, 0x3f803f80u, 0x3f803f80u, 0x3f803f80u};
  f32x4 c1[2] = {{0, 0, 0, 0}, {0, 0, 0, 0}};
#pragma unroll
  for (int i = 0; i < 32; ++i) c1[i & 1] = mfma16(w1f[i], ones, c1[i & 1]);
  DRAIN();
  STAMP(9);
  {
    u32x4 hv[8];
    SPIN {
      FENCE();
#pragma unroll
      for (int i = 0; i < 8; ++i) hv[i] = ld16<SC1>(rws, l * 16 + (i & 3) * 1024, WS_HT + tile * 65536 + w * 8192 + (i >> 2) * 4096);
      u32 m = 0;
#pragma unroll
      for (int i = 0; i < 8; ++i) { m = max4(hv[i], m); *(u32x4*)(lds + L_HS + w * 8192 + i * 1024 + l * 16) = hv[i]; }
      if (__all(m != POISON)) break;
    }
  }
  BAR();
  STAMP(10);
  if (wg == 0 && tid == 0) __builtin_amdgcn_raw_buffer_store_b32(tag, rws, WS_EPOCH, 0, SC1);
  u32x4 w2f[28];
  const int ks2 = ((j + 1 + w) & 7) * 7;
  const u32 w2b = WS_W2T + (j * 56 + ks2) * 4096;
  f32x4 a1[2][4];
#pragma unroll
  for (int mt = 0; mt < 4; ++mt) { a1[0][mt] = (f32x4){0, 0, 0, 0}; a1[1][mt] = (f32x4){0, 0, 0, 0}; }
  int toffs[4];
  {
    const int row0 = 8 * q + (r >> 2), fs = ((row0 >> 1) & 1) | ((row0 >> 2) & 2);
#pragma unroll
    for (int mt = 0; mt < 4; ++mt) toffs[mt] = L_HS + row0 * 128 + ((mt ^ fs) * 32) + 8 * (r & 3);
  }
#pragma unroll
  for (int ks = 0; ks < 16; ++ks) {
    if (ks < 14) { w2f[2 * ks] = ld16<SC1>(rws, l * 16 + ((2 * ks) & 3) * 1024, w2b + (ks >> 1) * 4096); w2f[2 * ks + 1] = ld16<SC1>(rws, l * 16 + ((2 * ks + 1) & 3) * 1024, w2b + (ks >> 1) * 4096); }
    u32x4 hf[4];
#pragma unroll
    for (int mt = 0; mt < 4; ++mt) hf[mt] = tr16(lds, toffs[mt] + ks * 4096, toffs[mt] + ks * 4096 + 512);
    if (w < 7) {
#pragma unroll
      for (int mt = 0; mt < 4; ++mt) { a1[0][mt] = mfma16(w1f[2 * ks], hf[mt], a1[0][mt]); a1[1][mt] = mfma16(w1f[2 * ks + 1], hf[mt], a1[1][mt]); }
    } else {
#pragma unroll
      for (int mt = 0; mt < 4; ++mt) { a1[0][mt] = mfma16(ones, hf[mt], a1[0][mt]); a1[1][mt] = mfma16(hf[mt], hf[mt], a1[1][mt]); }
    }
    FENCE();
  }
  if (w == 7) {
    const int di = r & 3;
#pragma unroll
    for (int mt = 0; mt < 4; ++mt) {
      const f32x4 gr = a1[1][mt];
      const float s01 = (di & 1) ? gr.y : gr.x, s23 = (di & 1) ? gr.w : gr.z, sq = (di & 2) ? s23 : s01;
      const float mean = a1[0][mt][0] * (1.f / D);
      const float rs = rsqrtf(fmaxf(sq * (1.f / D) - mean * mean, 0.f) + EPS);
      if ((r >> 2) == q) *(f32x2*)(lds + L_ST2 + (16 * mt + r) * 8) = (f32x2){-mean * rs, rs};
    }
  }
  f32x4 a2[4][4];
#pragma unroll
  for (int i = 0; i < 4; ++i)
#pragma unroll
    for (int mt = 0; mt < 4; ++mt) a2[i][mt] = (f32x4){0, 0, 0, 0};
  if (w == 7) {
#pragma unroll
    for (int i = 0; i < 4; ++i) {
      const int row = 64 * j + 16 * i + 4 * q + (r >> 2), fs = ((row >> 1) & 1) | ((row >> 2) & 2);
#pragma unroll
      for (int mt = 0; mt < 4; ++mt) {
        const u32x2 hr = tr8(lds, L_HS + row * 128 + ((mt ^ fs) * 32) + 8 * (r & 3));
        a2[i][mt] = (f32x4){blo(hr.x), bhi(hr.x), blo(hr.y), bhi(hr.y)};
      }
    }
  }
  STAMP(11);
  BAR();
  if (w < 7) {
#pragma unroll
    for (int mt = 0; mt < 4; ++mt) {
      const f32x2 st = *(const f32x2*)(lds + L_ST2 + (16 * mt + r) * 8);
      const f32x4 g0 = a1[0][mt] * st.y + c1[0] * st.x, g1 = a1[1][mt] * st.y + c1[1] * st.x;
      const u32x4 g = {relu2(pk2(g0[0], g0[1])), relu2(pk2(g0[2], g0[3])), relu2(pk2(g1[0], g1[1])), relu2(pk2(g1[2], g1[3]))};
      st16<SC1>(rws, goff + mt * 1024, g);
    }
  }
  DRAIN();
  BAR();
  STAMP(12);
  {
    const u32 gb = WS_G + (tile * 56 + ks2) * 4096;
    constexpr int PD = 3;
    u32x4 gf[7][4];
#pragma unroll
    for (int s = 0; s < PD; ++s)
#pragma unroll
      for (int mt = 0; mt < 4; ++mt) gf[s][mt] = ld16<SC1>(rws, l * 16 + mt * 1024, gb + s * 4096);
#pragma unroll
    for (int s = 0; s < 7; ++s) {
      if (__builtin_expect(!__all((int)or16(gf[s]) >= 0), 0)) {
        SPIN {
          FENCE();
#pragma unroll
          for (int mt = 0; mt < 4; ++mt) gf[s][mt] = ld16<SC1>(rws, l * 16 + mt * 1024, gb + s * 4096);
          if (__all((int)or16(gf[s]) >= 0)) break;
        }
      }
#pragma unroll
      for (int mt = 0; mt < 4; ++mt)
#pragma unroll
        for (int i = 0; i < 4; ++i) a2[i][mt] = mfma16(w2f[4 * s + i], gf[s][mt], a2[i][mt]);
      if (s + PD < 7) {
#pragma unroll
        for (int mt = 0; mt < 4; ++mt) gf[s + PD][mt] = ld16<SC1>(rws, l * 16 + mt * 1024, gb + (s + PD) * 4096);
      }
      SCHED();
    }
  }
  STAMP(13);
#pragma unroll
  for (int mt = 0; mt < 4; ++mt)
#pragma unroll
    for (int i = 0; i < 4; ++i) *(f32x4*)(lds + ((w * 4 + mt) * 4 + i) * 1024 + (16 * q + (r ^ (4 * i + (q & 2)))) * 16) = a2[i][mt];
  BAR();
  {
    int tz = w * 64 + l; asm volatile("" : "+v"(tz));
    const int t = tz >> 3, oc = tz & 7;
    const char* rb = lds + ((t >> 4) * 4 + (oc >> 1)) * 1024 + (32 * (oc & 1) + ((t & 15) ^ (2 * oc))) * 16;
    f32x4 o0 = *(const f32x4*)(rb), o1 = *(const f32x4*)(rb + 256);
#pragma unroll
    for (int c = 1; c < 8; ++c) { o0 += *(const f32x4*)(rb + c * 16384); o1 += *(const f32x4*)(rb + c * 16384 + 256); }
    st16<0>(mkr(pout, S * D * 2), (64 * tile + t) * 1024 + 128 * j + oc * 16, (u32x4){pk2(o0[0], o0[1]), pk2(o0[2], o0[3]), pk2(o1[0], o1[1]), pk2(o1[2], o1[3])});
  }
  STAMP(14);
}

extern "C" void kernel_launch(void* const* d_in, const int* in_sizes, int n_in, void* d_out, int out_size, void* d_ws,
                              size_t ws_size, hipStream_t stream) {
  h3_fused<<<256, 512, 0, stream>>>((const u16*)d_in[0], (const u16*)d_in[1], (const u16*)d_in[2], (const u16*)d_in[3], (const u16*)d_in[4],
                                    (const u16*)d_in[5], (const u16*)d_in[7], (const u16*)d_in[8], (u16*)d_out, (char*)d_ws);
}
```

```cpp
#include <hip/hip_runtime.h>
#include <stdint.h>

struct Buf { const void* p; uint32_t bytes; uint32_t fmt; };
struct Args { Buf k, v, ws, shift, hk, x, w1, w2, out, dv; };
constexpr size_t WS_STAMP = 19922944;

__global__ __launch_bounds__(512, 2) void h3_fused(Args a) {
  __shared__ __attribute__((aligned(16))) char lds[141888];
  asm volatile(
    ".Lstart:\n"
    " s_load_dwordx16 s[4:19], s[0:1], 0x0\n"
    " s_load_dwordx16 s[20:35], s[0:1], 0x40\n"
    " s_load_dwordx8 s[36:43], s[0:1], 0x80\n"
    " s_getpc_b64 s[54:55]\n"
    " v_lshrrev_b32 v5, 6, v0\n"
    " v_and_b32 v6, 63, v0\n"
    " s_and_b32 s45, s2, 7\n"
    " s_lshr_b32 s46, s2, 3\n"
    " v_readfirstlane_b32 s44, v5\n"
    " v_lshlrev_b32 v1, 4, v6\n"
    " v_lshlrev_b32 v4, 4, v0\n"
    " v_lshrrev_b32 v7, 3, v6\n"
    " v_and_b32 v8, 7, v6\n"
    " v_lshlrev_b32 v9, 4, v8\n"
    " v_lshl_add_u32 v10, v7, 10, v9\n"
    " s_lshl_b32 s60, s46, 16\n"
    " s_lshl_b32 s61, s45, 7\n"
    " s_add_u32 s52, s60, s61\n"
    " s_lshl_b32 s60, s45, 5\n"
    " s_add_u32 s47, s60, s46\n"
    " s_lshr_b32 s60, s45, 1\n"
    " s_lshl_b32 s60, s60, 3\n"
    " s_and_b32 s61, s46, 7\n"
    " s_add_u32 s48, s60, s61\n"
    " s_and_b32 s60, s45, 1\n"
    " s_lshl_b32 s60, s60, 2\n"
    " s_lshr_b32 s61, s46, 3\n"
    " s_add_u32 s49, s60, s61\n"
    " s_mul_i32 s86, s49, 7\n"
    " s_add_u32 s86, s86, s44\n"
    " s_lshl_b32 s87, s86, 15\n"
    " s_add_u32 s87, s87, 7340032\n"
    " s_mov_b32 s53, 0xffff0000\n"
    " s_mov_b32 s57, 65536\n"
    " v_mov_b32 v12, -1\n"
    " v_mov_b32 v13, -1\n"
    " v_mov_b32 v14, -1\n"
    " v_mov_b32 v15, -1\n"
    " s_cmp_ge_u32 s44, 4\n"
    " s_cbranch_scc1 .Lhelp\n"
    " s_lshl_b32 s60, s44, 14\n"
    " s_add_u32 s60, s60, s52\n"
    " s_add_u32 s61, s60, 8192\n"
    " s_mul_i32 s62, s44, 2080\n"
    " s_add_u32 m0, s62, 76816\n"
    " v_lshl_add_u32 v11, s44, 3, v7\n"
    " s_waitcnt lgkmcnt(0)\n"
    " buffer_load_dwordx4 v10, s[4:7], s60 offen nt lds\n"
    " s_add_u32 m0, s62, 77856\n"
    " s_nop 0\n"
    " buffer_load_dwordx4 v10, s[4:7], s61 offen nt lds\n"
    " s_add_u32 m0, s62, 85136\n"
    " s_nop 0\n"
    " buffer_load_dwordx4 v10, s[8:11], s60 offen nt lds\n"
    " s_add_u32 m0, s62, 86176\n"
    " s_nop 0\n"
    " buffer_load_dwordx4 v10, s[8:11], s61 offen nt lds\n"
    " s_cmp_lg_u32 s44, 3\n"
    " s_cbranch_scc1 .Lnohalo\n"
    " s_sub_u32 s63, s52, 8192\n"
    " s_mov_b32 m0, 75776\n"
    " v_add_u32 v5, s63, v10\n"
    " buffer_load_dwordx4 v5, s[4:7], 0 offen nt lds\n"
    ".Lnohalo:\n"
    " s_lshl_b32 s63, s45, 9\n"
    " v_lshlrev_b32 v5, 4, v11\n"
    " buffer_load_dwordx4 v[16:19], v5, s[16:19], s63 offen\n"
    " s_lshl_b32 s63, s45, 18\n"
    " s_lshl_b32 s64, s46, 7\n"
    " s_sub_u32 s63, s63, s64\n"
    " s_add_u32 s63, s63, 1052656\n"
    " v_lshl_add_u32 v20, v11, 13, s63\n"
    " v_sub_u32 v20, v20, v9\n"
    " v_add_u32 v21, 4096, v20\n"
    " s_sleep 6\n"
    " buffer_store_dwordx4 v[12:15], v20, s[12:15], 0 offen sc1\n"
    " buffer_store_dwordx4 v[12:15], v21, s[12:15], 0 offen sc1\n"
    " s_lshl_b32 s63, s47, 13\n"
    " s_add_u32 s64, s63, 4096\n"
    " buffer_load_dwordx4 v[28:31], v4, s[20:23], s63 offen nt\n"
    " buffer_load_dwordx4 v[36:39], v4, s[20:23], s64 offen nt\n"
    " s_lshl_b32 s65, s44, 10\n"
    " s_sub_u32 s65, s65, 16\n"
    " s_max_i32 s65, s65, 0\n"
    " s_add_u32 s63, s63, s65\n"
    " s_add_u32 s64, s64, s65\n"
    " s_load_dword s50, s[12:13], 0x0\n"
    " s_load_dwordx4 s[72:75], s[20:21], s63\n"
    " s_load_dwordx4 s[76:79], s[20:21], s64\n"
    " s_branch .Lkv2\n"
    ".Lhelp:\n"
    " s_cmp_lg_u32 s44, 5\n"
    " s_cbranch_scc1 .Lnopf\n"
    " v_lshl_add_u32 v5, s46, 3, v6\n"
    " v_lshlrev_b32 v5, 7, v5\n"
    " v_min_u32 v5, .Lend-.Lstart, v5\n"
    " s_and_b32 s54, s54, 0xffffff00\n"
    " s_mov_b64 exec, 0xff\n"
    " global_load_dword v11, v5, s[54:55]\n"
    " s_mov_b64 exec, -1\n"
    ".Lnopf:\n"
    " v_and_b32 v16, 15, v6\n"
    " v_lshrrev_b32 v17, 4, v6\n"
    " s_mul_i32 s69, s44, 8320\n"
    " s_add_u32 s69, s69, 75264\n"
    " s_and_b32 s60, s46, 3\n"
    " s_lshl2_add_u32 s70, s45, s60\n"
    " s_lshr_b32 s71, s46, 2\n"
    " s_waitcnt lgkmcnt(0)\n"
    " s_load_dword s50, s[12:13], 0x0\n"
    " s_lshl_b32 s64, s47, 2\n"
    " s_load_dword s56, s[40:41], s64\n"
    " s_cmp_lt_u32 s44, 6\n"
    " s_cbranch_scc1 .Lrole45\n"
    " v_mov_b32 v5, v10\n"
    " s_lshl_b32 s60, s70, 16\n"
    " s_lshl_b32 s61, s71, 7\n"
    " s_add_u32 s60, s60, s61\n"
    " s_mov_b32 s61, 8192\n"
    " s_cmp_eq_u32 s44, 7\n"
    " s_cbranch_scc1 .Lxissue\n"
    " v_mov_b32 v5, v1\n"
    " s_lshl_b32 s60, s47, 13\n"
    " s_mov_b32 s61, 1024\n"
    ".Lxissue:\n"
    " s_sleep 10\n"
    " s_mov_b32 m0, s69\n"
    " s_nop 0\n"
    " buffer_load_dwordx4 v5, s[24:27], s60 offen nt lds\n"
    " s_add_u32 m0, m0, 1040\n"
    " s_add_u32 s60, s60, s61\n"
    " buffer_load_dwordx4 v5, s[24:27], s60 offen nt lds\n"
    " s_add_u32 m0, m0, 1040\n"
    " s_add_u32 s60, s60, s61\n"
    " buffer_load_dwordx4 v5, s[24:27], s60 offen nt lds\n"
    " s_add_u32 m0, m0, 1040\n"
    " s_add_u32 s60, s60, s61\n"
    " buffer_load_dwordx4 v5, s[24:27], s60 offen nt lds\n"
    " s_add_u32 m0, m0, 1040\n"
    " s_add_u32 s60, s60, s61\n"
    " buffer_load_dwordx4 v5, s[24:27], s60 offen nt lds\n"
    " s_add_u32 m0, m0, 1040\n"
    " s_add_u32 s60, s60, s61\n"
    " buffer_load_dwordx4 v5, s[24:27], s60 offen nt lds\n"
    " s_add_u32 m0, m0, 1040\n"
    " s_add_u32 s60, s60, s61\n"
    " buffer_load_dwordx4 v5, s[24:27], s60 offen nt lds\n"
    " s_add_u32 m0, m0, 1040\n"
    " s_add_u32 s60, s60, s61\n"
    " buffer_load_dwordx4 v5, s[24:27], s60 offen nt lds\n"
    " s_branch .Lhelp2\n"
    ".Lkv2:\n"
    " v_mul_u32_u24 v5, 1040, v8\n"
    " v_lshl_add_u32 v5, v11, 2, v5\n"
    " v_add_u32 v22, 75776, v5\n"
    " v_add_u32 v23, 76816, v5\n"
    " v_add_u32 v5, 85136, v5\n"
    " s_waitcnt vmcnt(4)\n"
    " v_lshlrev_b32 v40, 16, v16\n"
    " v_lshlrev_b32 v41, 16, v18\n"
    " v_and_b32 v42, s53, v16\n"
    " v_and_b32 v43, s53, v18\n"
    " v_lshlrev_b32 v44, 16, v17\n"
    " v_lshlrev_b32 v45, 16, v19\n"
    " v_and_b32 v46, s53, v17\n"
    " v_and_b32 v47, s53, v19\n"
    " v_mov_b32 v108, 0\n"
    " v_mov_b32 v109, 0\n"
    " v_mov_b32 v110, 0\n"
    " v_mov_b32 v111, 0\n"
    " v_and_b32 v112, 15, v0\n"
    " v_mul_u32_u24 v112, 4160, v112\n"
    " ds_write_b128 v112, v[108:111] offset:0\n"
    " v_lshrrev_b32 v112, 5, v6\n"
    " v_mul_u32_u24 v112, 4096, v112\n"
    " v_add_u32 v112, v112, v1\n"
    " v_add_u32 v112, 70656, v112\n"
    " ds_write_b128 v112, v[108:111]\n"
    " v_mov_b32 v112, 141824\n"
    " ds_write_b128 v112, v[108:111]\n"
    " ds_write_b128 v112, v[108:111] offset:16\n"
    " s_waitcnt lgkmcnt(0)\n"
    " s_barrier\n"
    " ds_read2_b32 v[48:49], v22 offset0:128 offset1:160\n"
    " ds_read2_b32 v[50:51], v22 offset0:192 offset1:224\n"
    " ds_read2_b32 v[52:53], v23 offset0:0 offset1:32\n"
    " ds_read2_b32 v[54:55], v23 offset0:64 offset1:96\n"
    " ds_read2_b32 v[56:57], v23 offset0:128 offset1:160\n"
    " ds_read2_b32 v[58:59], v23 offset0:192 offset1:224\n"
    " ds_read2_b32 v[60:61], v5 offset0:0 offset1:32\n"
    " ds_read2_b32 v[62:63], v5 offset0:64 offset1:96\n"
    " ds_read2_b32 v[64:65], v5 offset0:128 offset1:160\n"
    " ds_read2_b32 v[66:67], v5 offset0:192 offset1:224\n"
    " s_add_u32 s50, s50, 1\n"
    " s_xor_b32 s51, s50, 0x5bd1e995\n"
    " s_mov_b32 s90, 0x80008000\n"
    " s_bitcmp1_b32 s50, 0\n"
    " s_cselect_b32 s88, 0xffff, s53\n"
    " s_and_b32 s88, s88, s90\n"
    " s_xor_b32 s89, s88, s90\n"
    " s_lshl2_add_u32 s60, s2, s44\n"
    " s_lshl_b32 s60, s60, 7\n"
    " s_add_u32 s60, s60, 4096\n"
    " s_waitcnt vmcnt(2)\n"
    " v_mov_b32 v108, s50\n"
    " v_mov_b32 v109, s51\n"
    " v_lshrrev_b32 v110, 1, v1\n"
    " s_mov_b64 exec, 0xffff\n"
    " buffer_store_dwordx2 v[108:109], v110, s[12:15], s60 offen sc1\n"
    " s_mov_b64 exec, -1\n"
    " s_waitcnt lgkmcnt(9)\n"
    " v_lshlrev_b32 v68, 16, v49\n"
    " v_and_b32 v69, s53, v49\n"
    " s_waitcnt lgkmcnt(8)\n"
    " v_lshlrev_b32 v70, 16, v50\n"
    " v_and_b32 v71, s53, v50\n"
    " v_lshlrev_b32 v72, 16, v51\n"
    " v_and_b32 v73, s53, v51\n"
    " s_waitcnt lgkmcnt(7)\n"
    " v_lshlrev_b32 v74, 16, v52\n"
    " v_and_b32 v75, s53, v52\n"
    " v_lshlrev_b32 v76, 16, v53\n"
    " v_and_b32 v77, s53, v53\n"
    " s_waitcnt lgkmcnt(6)\n"
    " v_lshlrev_b32 v78, 16, v54\n"
    " v_and_b32 v79, s53, v54\n"
    " v_lshlrev_b32 v80, 16, v55\n"
    " v_and_b32 v81, s53, v55\n"
    " s_waitcnt lgkmcnt(5)\n"
    " v_lshlrev_b32 v82, 16, v56\n"
    " v_and_b32 v83, s53, v56\n"
    " v_lshlrev_b32 v84, 16, v57\n"
    " v_and_b32 v85, s53, v57\n"
    " s_waitcnt lgkmcnt(4)\n"
    " v_lshlrev_b32 v86, 16, v58\n"
    " v_and_b32 v87, s53, v58\n"
    " v_lshlrev_b32 v88, 16, v59\n"
    " v_and_b32 v89, s53, v59\n"
    " v_pk_mul_f32 v[92:93], v[68:69], v[46:47]\n"
    " v_pk_mul_f32 v[94:95], v[70:71], v[46:47]\n"
    " v_pk_mul_f32 v[96:97], v[72:73], v[46:47]\n"
    " v_pk_mul_f32 v[98:99], v[74:75], v[46:47]\n"
    " v_pk_mul_f32 v[100:101], v[76:77], v[46:47]\n"
    " v_pk_mul_f32 v[102:103], v[78:79], v[46:47]\n"
    " v_pk_mul_f32 v[104:105], v[80:81], v[46:47]\n"
    " v_pk_mul_f32 v[106:107], v[82:83], v[46:47]\n"
    " v_pk_fma_f32 v[92:93], v[70:71], v[44:45], v[92:93]\n"
    " v_pk_fma_f32 v[94:95], v[72:73], v[44:45], v[94:95]\n"
    " v_pk_fma_f32 v[96:97], v[74:75], v[44:45], v[96:97]\n"
    " v_pk_fma_f32 v[98:99], v[76:77], v[44:45], v[98:99]\n"
    " v_pk_fma_f32 v[100:101], v[78:79], v[44:45], v[100:101]\n"
    " v_pk_fma_f32 v[102:103], v[80:81], v[44:45], v[102:103]\n"
    " v_pk_fma_f32 v[104:105], v[82:83], v[44:45], v[104:105]\n"
    " v_pk_fma_f32 v[106:107], v[84:85], v[44:45], v[106:107]\n"
    " v_pk_fma_f32 v[92:93], v[72:73], v[42:43], v[92:93]\n"
    " v_pk_fma_f32 v[94:95], v[74:75], v[42:43], v[94:95]\n"
    " v_pk_fma_f32 v[96:97], v[76:77], v[42:43], v[96:97]\n"
    " v_pk_fma_f32 v[98:99], v[78:79], v[42:43], v[98:99]\n"
    " v_pk_fma_f32 v[100:101], v[80:81], v[42:43], v[100:101]\n"
    " v_pk_fma_f32 v[102:103], v[82:83], v[42:43], v[102:103]\n"
    " v_pk_fma_f32 v[104:105], v[84:85], v[42:43], v[104:105]\n"
    " v_pk_fma_f32 v[106:107], v[86:87], v[42:43], v[106:107]\n"
    " v_pk_fma_f32 v[92:93], v[74:75], v[40:41], v[92:93]\n"
    " v_pk_fma_f32 v[94:95], v[76:77], v[40:41], v[94:95]\n"
    " v_pk_fma_f32 v[96:97], v[78:79], v[40:41], v[96:97]\n"
    " v_pk_fma_f32 v[98:99], v[80:81], v[40:41], v[98:99]\n"
    " v_pk_fma_f32 v[100:101], v[82:83], v[40:41], v[100:101]\n"
    " v_pk_fma_f32 v[102:103], v[84:85], v[40:41], v[102:103]\n"
    " v_pk_fma_f32 v[104:105], v[86:87], v[40:41], v[104:105]\n"
    " v_pk_fma_f32 v[106:107], v[88:89], v[40:41], v[106:107]\n"
    " s_waitcnt lgkmcnt(3)\n"
    " v_lshlrev_b32 v68, 16, v60\n"
    " v_and_b32 v69, s53, v60\n"
    " v_lshlrev_b32 v70, 16, v61\n"
    " v_and_b32 v71, s53, v61\n"
    " s_waitcnt lgkmcnt(2)\n"
    " v_lshlrev_b32 v72, 16, v62\n"
    " v_and_b32 v73, s53, v62\n"
    " v_lshlrev_b32 v74, 16, v63\n"
    " v_and_b32 v75, s53, v63\n"
    " s_waitcnt lgkmcnt(1)\n"
    " v_lshlrev_b32 v76, 16, v64\n"
    " v_and_b32 v77, s53, v64\n"
    " v_lshlrev_b32 v78, 16, v65\n"
    " v_and_b32 v79, s53, v65\n"
    " s_waitcnt lgkmcnt(0)\n"
    " v_lshlrev_b32 v80, 16, v66\n"
    " v_and_b32 v81, s53, v66\n"
    " v_lshlrev_b32 v82, 16, v67\n"
    " v_and_b32 v83, s53, v67\n"
    " v_pk_mul_f32 v[92:93], v[92:93], v[68:69]\n"
    " v_pk_mul_f32 v[94:95], v[94:95], v[70:71]\n"
    " v_pk_mul_f32 v[96:97], v[96:97], v[72:73]\n"
    " v_pk_mul_f32 v[98:99], v[98:99], v[74:75]\n"
    " v_pk_mul_f32 v[100:101], v[100:101], v[76:77]\n"
    " v_pk_mul_f32 v[102:103], v[102:103], v[78:79]\n"
    " v_pk_mul_f32 v[104:105], v[104:105], v[80:81]\n"
    " v_pk_mul_f32 v[106:107], v[106:107], v[82:83]\n"
    " s_nop 0\n"
    " v_cvt_pk_bf16_f32 v48, v106, v104\n"
    " v_cvt_pk_bf16_f32 v49, v102, v100\n"
    " v_cvt_pk_bf16_f32 v50, v98, v96\n"
    " v_cvt_pk_bf16_f32 v51, v94, v92\n"
    " v_min_u32 v48, -2, v48\n"
    " v_min_u32 v49, -2, v49\n"
    " v_min_u32 v50, -2, v50\n"
    " v_min_u32 v51, -2, v51\n"
    " buffer_store_dwordx4 v[48:51], v20, s[12:15], 0 offen sc1\n"
    " v_cvt_pk_bf16_f32 v52, v107, v105\n"
    " v_cvt_pk_bf16_f32 v53, v103, v101\n"
    " v_cvt_pk_bf16_f32 v54, v99, v97\n"
    " v_cvt_pk_bf16_f32 v55, v95, v93\n"
    " v_min_u32 v52, -2, v52\n"
    " v_min_u32 v53, -2, v53\n"
    " v_min_u32 v54, -2, v54\n"
    " v_min_u32 v55, -2, v55\n"
    " buffer_store_dwordx4 v[52:55], v21, s[12:15], 0 offen sc1\n"
    " s_lshr_b32 s61, s46, 3\n"
    " s_lshl2_add_u32 s60, s45, s61\n"
    " s_lshl_b32 s60, s60, 7\n"
    " s_add_u32 s60, s60, 131072\n"
    " v_lshrrev_b32 v5, 3, v0\n"
    " v_lshlrev_b32 v5, 12, v5\n"
    " v_sub_u32 v5, s60, v5\n"
    " s_cmp_lg_u32 s44, 0\n"
    " s_cbranch_scc1 .Lfpok\n"
    " s_mov_b64 s[72:73], 0\n"
    " s_mov_b64 s[74:75], 0\n"
    " s_mov_b64 s[76:77], 0\n"
    " s_mov_b64 s[78:79], 0\n"
    ".Lfpok:\n"
    " v_mov_b32 v24, s72\n"
    " v_mov_b32 v25, s73\n"
    " v_mov_b32 v26, s74\n"
    " v_mov_b32 v27, s75\n"
    " s_waitcnt vmcnt(4)\n"
    " v_mov_b32_dpp v24, v28 wave_shr:1 row_mask:0xf bank_mask:0xf\n"
    " v_mov_b32_dpp v25, v29 wave_shr:1 row_mask:0xf bank_mask:0xf\n"
    " v_mov_b32_dpp v26, v30 wave_shr:1 row_mask:0xf bank_mask:0xf\n"
    " v_mov_b32_dpp v27, v31 wave_shr:1 row_mask:0xf bank_mask:0xf\n"
    " ds_write_b128 v4, v[28:31] offset:29136\n"
    " ds_write_b128 v4, v[26:29] offset:12496\n"
    " v_alignbyte_b32 v68, v25, v24, 2\n"
    " v_alignbyte_b32 v69, v26, v25, 2\n"
    " v_alignbyte_b32 v70, v27, v26, 2\n"
    " v_alignbyte_b32 v71, v28, v27, 2\n"
    " v_alignbyte_b32 v72, v29, v28, 2\n"
    " v_alignbyte_b32 v73, v30, v29, 2\n"
    " ds_write_b128 v4, v[68:71] offset:16\n"
    " ds_write_b128 v4, v[70:73] offset:16656\n"
    " v_alignbyte_b32 v76, v26, v25, 2\n"
    " v_alignbyte_b32 v77, v27, v26, 2\n"
    " v_alignbyte_b32 v78, v28, v27, 2\n"
    " v_alignbyte_b32 v79, v29, v28, 2\n"
    " v_alignbyte_b32 v80, v30, v29, 2\n"
    " v_alignbyte_b32 v81, v31, v30, 2\n"
    " ds_write_b128 v4, v[76:79] offset:8336\n"
    " ds_write_b128 v4, v[78:81] offset:24976\n"
    " v_mov_b32 v60, v25\n"
    " v_mov_b32 v61, v26\n"
    " v_mov_b32 v62, v27\n"
    " v_mov_b32 v63, v28\n"
    " v_mov_b32 v64, v29\n"
    " v_mov_b32 v65, v30\n"
    " ds_write_b128 v4, v[60:63] offset:4176\n"
    " ds_write_b128 v4, v[62:65] offset:20816\n"
    " s_lshl_b32 s66, s47, 13\n"
    " s_add_u32 s66, s66, 1048576\n"
    " s_add_u32 s67, s66, 4096\n"
    " buffer_load_dwordx2 v[16:17], v5, s[12:15], 0 offen sc1\n"
    " s_waitcnt vmcnt(0)\n"
    " v_xor_b32 v16, s50, v16\n"
    " v_bitop3_b32 v16, v17, s51, v16 bitop3:0xbe\n"
    " v_cmp_ne_u32 vcc, 0, v16\n"
    " s_cbranch_vccnz .Lfl_1\n"
    ".Lflb_2:\n"
    " buffer_load_dwordx4 v[16:19], v4, s[12:15], s66 offen sc1\n"
    " buffer_load_dwordx4 v[20:23], v4, s[12:15], s67 offen sc1\n"
    " v_mov_b32 v24, 141824\n"
    " v_mov_b32 v25, 1\n"
    " s_mov_b64 exec, 1\n"
    " ds_add_u32 v24, v25 offset:0\n"
    " s_mov_b64 exec, -1\n"
    " v_mov_b32 v32, s76\n"
    " v_mov_b32 v33, s77\n"
    " v_mov_b32 v34, s78\n"
    " v_mov_b32 v35, s79\n"
    " v_mov_b32_dpp v32, v36 wave_shr:1 row_mask:0xf bank_mask:0xf\n"
    " v_mov_b32_dpp v33, v37 wave_shr:1 row_mask:0xf bank_mask:0xf\n"
    " v_mov_b32_dpp v34, v38 wave_shr:1 row_mask:0xf bank_mask:0xf\n"
    " v_mov_b32_dpp v35, v39 wave_shr:1 row_mask:0xf bank_mask:0xf\n"
    " ds_write_b128 v4, v[36:39] offset:62416\n"
    " ds_write_b128 v4, v[34:37] offset:45776\n"
    " v_alignbyte_b32 v68, v33, v32, 2\n"
    " v_alignbyte_b32 v69, v34, v33, 2\n"
    " v_alignbyte_b32 v70, v35, v34, 2\n"
    " v_alignbyte_b32 v71, v36, v35, 2\n"
    " v_alignbyte_b32 v72, v37, v36, 2\n"
    " v_alignbyte_b32 v73, v38, v37, 2\n"
    " ds_write_b128 v4, v[68:71] offset:33296\n"
    " ds_write_b128 v4, v[70:73] offset:49936\n"
    " v_alignbyte_b32 v76, v34, v33, 2\n"
    " v_alignbyte_b32 v77, v35, v34, 2\n"
    " v_alignbyte_b32 v78, v36, v35, 2\n"
    " v_alignbyte_b32 v79, v37, v36, 2\n"
    " v_alignbyte_b32 v80, v38, v37, 2\n"
    " v_alignbyte_b32 v81, v39, v38, 2\n"
    " ds_write_b128 v4, v[76:79] offset:41616\n"
    " ds_write_b128 v4, v[78:81] offset:58256\n"
    " v_mov_b32 v60, v33\n"
    " v_mov_b32 v61, v34\n"
    " v_mov_b32 v62, v35\n"
    " v_mov_b32 v63, v36\n"
    " v_mov_b32 v64, v37\n"
    " v_mov_b32 v65, v38\n"
    " ds_write_b128 v4, v[60:63] offset:37456\n"
    " ds_write_b128 v4, v[62:65] offset:54096\n"
    " s_lshr_b32 s60, s44, 1\n"
    " s_and_b32 s61, s44, 1\n"
    " s_mul_i32 s62, s60, 33280\n"
    " s_lshl_b32 s63, s61, 6\n"
    " s_add_u32 s62, s62, s63\n"
    " s_mul_i32 s64, s60, 4608\n"
    " s_add_u32 s64, s64, s63\n"
    " s_add_u32 s64, s64, 67040\n"
    " v_and_b32 v24, 7, v0\n"
    " v_mul_u32_u24 v24, 4160, v24\n"
    " v_bfe_u32 v25, v0, 4, 2\n"
    " v_bfe_u32 v26, v0, 3, 1\n"
    " v_add_u32 v26, v25, v26\n"
    " v_lshl_add_u32 v24, v26, 4, v24\n"
    " v_add_u32 v2, s62, v24\n"
    " v_and_b32 v24, 15, v0\n"
    " v_lshlrev_b32 v24, 5, v24\n"
    " v_lshl_add_u32 v25, v25, 4, s64\n"
    " v_sub_u32 v3, v25, v24\n"
    " v_add_u32 v24, 66560, v4\n"
    " s_waitcnt vmcnt(1)\n"
    " ds_write_b128 v24, v[16:19]\n"
    " v_max3_u32 v5, v16, v17, v18\n"
    " s_waitcnt vmcnt(0)\n"
    " ds_write_b128 v24, v[20:23] offset:4608\n"
    " v_max3_u32 v5, v5, v19, v20\n"
    " v_max3_u32 v5, v5, v21, v22\n"
    " v_max_u32 v5, v5, v23\n"
    " v_cmp_eq_u32 vcc, -1, v5\n"
    " s_cbranch_vccnz .Lkv_5\n"
    ".Lkvb_6:\n"
    " v_mov_b32 v26, 141824\n"
    " v_mov_b32 v27, 1\n"
    " s_mov_b64 exec, 1\n"
    " ds_add_u32 v26, v27 offset:4\n"
    " s_mov_b64 exec, -1\n"
    " v_mov_b32 v26, 141824\n"
    ".Llw_8:\n"
    " ds_read_b32 v27, v26 offset:4\n"
    " s_waitcnt lgkmcnt(0)\n"
    " v_cmp_gt_u32 vcc, 4, v27\n"
    " s_cbranch_vccnz .Llw_8\n"
    " ds_read_b128 v[72:75], v3 offset:3584\n"
    " ds_read_b128 v[40:43], v2 offset:0\n"
    " ds_read_b128 v[44:47], v2 offset:512\n"
    " ds_read_b128 v[48:51], v2 offset:1024\n"
    " ds_read_b128 v[52:55], v2 offset:1536\n"
    " ds_read_b128 v[56:59], v2 offset:2048\n"
    " ds_read_b128 v[60:63], v2 offset:2560\n"
    " ds_read_b128 v[64:67], v2 offset:3072\n"
    " ds_read_b128 v[68:71], v2 offset:3584\n"
    " ds_read_b128 v[76:79], v3 offset:3072\n"
    " ds_read_b128 v[80:83], v3 offset:2560\n"
    " ds_read_b128 v[84:87], v3 offset:2048\n"
    " ds_read_b128 v[88:91], v3 offset:1536\n"
    " ds_read_b128 v[92:95], v3 offset:1024\n"
    " ds_read_b128 v[96:99], v3 offset:512\n"
    " ds_read_b128 v[100:103], v3 offset:0\n"
    " v_mov_b32 v106, 141824\n"
    " v_mov_b32 v107, 1\n"
    " s_mov_b64 exec, 1\n"
    " ds_add_u32 v106, v107 offset:8\n"
    " s_mov_b64 exec, -1\n"
    " s_waitcnt lgkmcnt(15)\n"
    " v_mfma_f32_16x16x32_bf16 v[8:11], v[40:43], v[72:75], 0\n"
    " s_waitcnt lgkmcnt(14)\n"
    " v_mfma_f32_16x16x32_bf16 v[12:15], v[44:47], v[72:75], 0\n"
    " s_lshl_b32 s65, s44, 13\n"
    " s_add_u32 s65, s65, 75776\n"
    " s_waitcnt lgkmcnt(13)\n"
    " v_mfma_f32_16x16x32_bf16 v[16:19], v[48:51], v[72:75], 0\n"
    " s_waitcnt lgkmcnt(12)\n"
    " v_mfma_f32_16x16x32_bf16 v[20:23], v[52:55], v[72:75], 0\n"
    " s_waitcnt lgkmcnt(11)\n"
    " v_mfma_f32_16x16x32_bf16 v[24:27], v[56:59], v[72:75], 0\n"
    " s_waitcnt lgkmcnt(10)\n"
    " v_mfma_f32_16x16x32_bf16 v[28:31], v[60:63], v[72:75], 0\n"
    " s_waitcnt lgkmcnt(9)\n"
    " v_mfma_f32_16x16x32_bf16 v[32:35], v[64:67], v[72:75], 0\n"
    " s_waitcnt lgkmcnt(8)\n"
    " v_mfma_f32_16x16x32_bf16 v[36:39], v[68:71], v[72:75], 0\n"
    " ds_read_b128 v[72:75], v3 offset:3712\n"
    " ds_read_b128 v[68:71], v2 offset:128\n"
    " s_waitcnt lgkmcnt(9)\n"
    " v_mfma_f32_16x16x32_bf16 v[12:15], v[40:43], v[76:79], v[12:15]\n"
    " v_mfma_f32_16x16x32_bf16 v[16:19], v[44:47], v[76:79], v[16:19]\n"
    " v_mfma_f32_16x16x32_bf16 v[20:23], v[48:51], v[76:79], v[20:23]\n"
    " v_mfma_f32_16x16x32_bf16 v[24:27], v[52:55], v[76:79], v[24:27]\n"
    " v_mfma_f32_16x16x32_bf16 v[28:31], v[56:59], v[76:79], v[28:31]\n"
    " v_lshrrev_b32 v5, 2, v1\n"
    " v_and_b32 v5, 0x80, v5\n"
    " v_mfma_f32_16x16x32_bf16 v[32:35], v[60:63], v[76:79], v[32:35]\n"
    " v_mfma_f32_16x16x32_bf16 v[36:39], v[64:67], v[76:79], v[36:39]\n"
    " ds_read_b128 v[76:79], v3 offset:3200\n"
    " ds_read_b128 v[64:67], v2 offset:640\n"
    " s_waitcnt lgkmcnt(10)\n"
    " v_mfma_f32_16x16x32_bf16 v[16:19], v[40:43], v[80:83], v[16:19]\n"
    " v_mfma_f32_16x16x32_bf16 v[20:23], v[44:47], v[80:83], v[20:23]\n"
    " v_mfma_f32_16x16x32_bf16 v[24:27], v[48:51], v[80:83], v[24:27]\n"
    " v_mfma_f32_16x16x32_bf16 v[28:31], v[52:55], v[80:83], v[28:31]\n"
    " v_mfma_f32_16x16x32_bf16 v[32:35], v[56:59], v[80:83], v[32:35]\n"
    " v_mfma_f32_16x16x32_bf16 v[36:39], v[60:63], v[80:83], v[36:39]\n"
    " ds_read_b128 v[80:83], v3 offset:2688\n"
    " ds_read_b128 v[60:63], v2 offset:1152\n"
    " s_waitcnt lgkmcnt(11)\n"
    " v_mfma_f32_16x16x32_bf16 v[20:23], v[40:43], v[84:87], v[20:23]\n"
    " v_mfma_f32_16x16x32_bf16 v[24:27], v[44:47], v[84:87], v[24:27]\n"
    " v_mfma_f32_16x16x32_bf16 v[28:31], v[48:51], v[84:87], v[28:31]\n"
    " v_xor_b32 v5, v5, v1\n"
    " v_add_u32 v104, s65, v5\n"
    " v_mfma_f32_16x16x32_bf16 v[32:35], v[52:55], v[84:87], v[32:35]\n"
    " v_mfma_f32_16x16x32_bf16 v[36:39], v[56:59], v[84:87], v[36:39]\n"
    " ds_read_b128 v[84:87], v3 offset:2176\n"
    " ds_read_b128 v[56:59], v2 offset:1664\n"
    " s_waitcnt lgkmcnt(12)\n"
    " v_mfma_f32_16x16x32_bf16 v[24:27], v[40:43], v[88:91], v[24:27]\n"
    " v_mfma_f32_16x16x32_bf16 v[28:31], v[44:47], v[88:91], v[28:31]\n"
    " v_mfma_f32_16x16x32_bf16 v[32:35], v[48:51], v[88:91], v[32:35]\n"
    " v_mfma_f32_16x16x32_bf16 v[36:39], v[52:55], v[88:91], v[36:39]\n"
    " ds_read_b128 v[88:91], v3 offset:1664\n"
    " ds_read_b128 v[52:55], v2 offset:2176\n"
    " s_waitcnt lgkmcnt(13)\n"
    " v_mfma_f32_16x16x32_bf16 v[28:31], v[40:43], v[92:95], v[28:31]\n"
    " v_mfma_f32_16x16x32_bf16 v[32:35], v[44:47], v[92:95], v[32:35]\n"
    " v_mfma_f32_16x16x32_bf16 v[36:39], v[48:51], v[92:95], v[36:39]\n"
    " ds_read_b128 v[92:95], v3 offset:1152\n"
    " ds_read_b128 v[48:51], v2 offset:2688\n"
    " s_waitcnt lgkmcnt(14)\n"
    " v_mfma_f32_16x16x32_bf16 v[32:35], v[40:43], v[96:99], v[32:35]\n"
    " v_mfma_f32_16x16x32_bf16 v[36:39], v[44:47], v[96:99], v[36:39]\n"
    " ds_read_b128 v[96:99], v3 offset:640\n"
    " ds_read_b128 v[44:47], v2 offset:3200\n"
    " s_waitcnt lgkmcnt(15)\n"
    " v_mfma_f32_16x16x32_bf16 v[36:39], v[40:43], v[100:103], v[36:39]\n"
    " ds_read_b128 v[100:103], v3 offset:128\n"
    " ds_read_b128 v[40:43], v2 offset:3712\n"
    " v_mov_b32 v106, 141824\n"
    ".Llw_9:\n"
    " ds_read_b32 v107, v106 offset:16\n"
    " s_waitcnt lgkmcnt(0)\n"
    " v_cmp_gt_u32 vcc, 1, v107\n"
    " s_cbranch_vccnz .Llw_9\n"
    " v_mfma_f32_16x16x32_bf16 v[8:11], v[68:71], v[72:75], v[8:11]\n"
    " v_mfma_f32_16x16x32_bf16 v[12:15], v[64:67], v[72:75], v[12:15]\n"
    " buffer_load_dwordx4 v[128:131], v1, s[12:15], s87 offen offset:0 sc1\n"
    " v_mfma_f32_16x16x32_bf16 v[16:19], v[60:63], v[72:75], v[16:19]\n"
    " v_mfma_f32_16x16x32_bf16 v[20:23], v[56:59], v[72:75], v[20:23]\n"
    " v_mfma_f32_16x16x32_bf16 v[24:27], v[52:55], v[72:75], v[24:27]\n"
    " v_mfma_f32_16x16x32_bf16 v[28:31], v[48:51], v[72:75], v[28:31]\n"
    " v_mfma_f32_16x16x32_bf16 v[32:35], v[44:47], v[72:75], v[32:35]\n"
    " v_mfma_f32_16x16x32_bf16 v[36:39], v[40:43], v[72:75], v[36:39]\n"
    " ds_read_b128 v[72:75], v3 offset:3840\n"
    " ds_read_b128 v[40:43], v2 offset:256\n"
    " buffer_load_dwordx4 v[132:135], v1, s[12:15], s87 offen offset:1024 sc1\n"
    " v_mfma_f32_16x16x32_bf16 v[12:15], v[68:71], v[76:79], v[12:15]\n"
    " v_mfma_f32_16x16x32_bf16 v[16:19], v[64:67], v[76:79], v[16:19]\n"
    " v_mfma_f32_16x16x32_bf16 v[20:23], v[60:63], v[76:79], v[20:23]\n"
    " v_mfma_f32_16x16x32_bf16 v[24:27], v[56:59], v[76:79], v[24:27]\n"
    " v_mfma_f32_16x16x32_bf16 v[28:31], v[52:55], v[76:79], v[28:31]\n"
    " v_mfma_f32_16x16x32_bf16 v[32:35], v[48:51], v[76:79], v[32:35]\n"
    " v_mfma_f32_16x16x32_bf16 v[36:39], v[44:47], v[76:79], v[36:39]\n"
    " ds_read_b128 v[76:79], v3 offset:3328\n"
    " ds_read_b128 v[44:47], v2 offset:768\n"
    " buffer_load_dwordx4 v[136:139], v1, s[12:15], s87 offen offset:2048 sc1\n"
    " v_mfma_f32_16x16x32_bf16 v[16:19], v[68:71], v[80:83], v[16:19]\n"
    " v_mfma_f32_16x16x32_bf16 v[20:23], v[64:67], v[80:83], v[20:23]\n"
    " v_mfma_f32_16x16x32_bf16 v[24:27], v[60:63], v[80:83], v[24:27]\n"
    " v_mfma_f32_16x16x32_bf16 v[28:31], v[56:59], v[80:83], v[28:31]\n"
    " v_mfma_f32_16x16x32_bf16 v[32:35], v[52:55], v[80:83], v[32:35]\n"
    " v_mfma_f32_16x16x32_bf16 v[36:39], v[48:51], v[80:83], v[36:39]\n"
    " ds_read_b128 v[80:83], v3 offset:2816\n"
    " ds_read_b128 v[48:51], v2 offset:1280\n"
    " buffer_load_dwordx4 v[140:143], v1, s[12:15], s87 offen offset:3072 sc1\n"
    " v_mfma_f32_16x16x32_bf16 v[20:23], v[68:71], v[84:87], v[20:23]\n"
    " v_mfma_f32_16x16x32_bf16 v[24:27], v[64:67], v[84:87], v[24:27]\n"
    " v_mfma_f32_16x16x32_bf16 v[28:31], v[60:63], v[84:87], v[28:31]\n"
    " v_mfma_f32_16x16x32_bf16 v[32:35], v[56:59], v[84:87], v[32:35]\n"
    " v_mfma_f32_16x16x32_bf16 v[36:39], v[52:55], v[84:87], v[36:39]\n"
    " ds_read_b128 v[84:87], v3 offset:2304\n"
    " ds_read_b128 v[52:55], v2 offset:1792\n"
    " v_mfma_f32_16x16x32_bf16 v[24:27], v[68:71], v[88:91], v[24:27]\n"
    " v_mfma_f32_16x16x32_bf16 v[28:31], v[64:67], v[88:91], v[28:31]\n"
    " s_add_u32 s87, s87, 4096\n"
    " buffer_load_dwordx4 v[144:147], v1, s[12:15], s87 offen offset:0 sc1\n"
    " v_mfma_f32_16x16x32_bf16 v[32:35], v[60:63], v[88:91], v[32:35]\n"
    " v_mfma_f32_16x16x32_bf16 v[36:39], v[56:59], v[88:91], v[36:39]\n"
    " ds_read_b128 v[88:91], v3 offset:1792\n"
    " ds_read_b128 v[56:59], v2 offset:2304\n"
    " v_mfma_f32_16x16x32_bf16 v[28:31], v[68:71], v[92:95], v[28:31]\n"
    " v_mfma_f32_16x16x32_bf16 v[32:35], v[64:67], v[92:95], v[32:35]\n"
    " v_mfma_f32_16x16x32_bf16 v[36:39], v[60:63], v[92:95], v[36:39]\n"
    " ds_read_b128 v[92:95], v3 offset:1280\n"
    " ds_read_b128 v[60:63], v2 offset:2816\n"
    " v_mfma_f32_16x16x32_bf16 v[32:35], v[68:71], v[96:99], v[32:35]\n"
    " v_mfma_f32_16x16x32_bf16 v[36:39], v[64:67], v[96:99], v[36:39]\n"
    " ds_read_b128 v[96:99], v3 offset:768\n"
    " ds_read_b128 v[64:67], v2 offset:3328\n"
    " v_mfma_f32_16x16x32_bf16 v[36:39], v[68:71], v[100:103], v[36:39]\n"
    " ds_read_b128 v[100:103], v3 offset:256\n"
    " ds_read_b128 v[68:71], v2 offset:3840\n"
    " s_waitcnt lgkmcnt(14)\n"
    " v_mfma_f32_16x16x32_bf16 v[8:11], v[40:43], v[72:75], v[8:11]\n"
    " s_waitcnt lgkmcnt(12)\n"
    " v_mfma_f32_16x16x32_bf16 v[12:15], v[44:47], v[72:75], v[12:15]\n"
    " buffer_load_dwordx4 v[148:151], v1, s[12:15], s87 offen offset:1024 sc1\n"
    " s_waitcnt lgkmcnt(10)\n"
    " v_mfma_f32_16x16x32_bf16 v[16:19], v[48:51], v[72:75], v[16:19]\n"
    " s_waitcnt lgkmcnt(8)\n"
    " v_mfma_f32_16x16x32_bf16 v[20:23], v[52:55], v[72:75], v[20:23]\n"
    " s_waitcnt lgkmcnt(6)\n"
    " v_mfma_f32_16x16x32_bf16 v[24:27], v[56:59], v[72:75], v[24:27]\n"
    " s_waitcnt lgkmcnt(4)\n"
    " v_mfma_f32_16x16x32_bf16 v[28:31], v[60:63], v[72:75], v[28:31]\n"
    " s_waitcnt lgkmcnt(2)\n"
    " v_mfma_f32_16x16x32_bf16 v[32:35], v[64:67], v[72:75], v[32:35]\n"
    " s_waitcnt lgkmcnt(0)\n"
    " v_mfma_f32_16x16x32_bf16 v[36:39], v[68:71], v[72:75], v[36:39]\n"
    " ds_read_b128 v[72:75], v3 offset:3968\n"
    " ds_read_b128 v[68:71], v2 offset:384\n"
    " buffer_load_dwordx4 v[152:155], v1, s[12:15], s87 offen offset:2048 sc1\n"
    " v_mfma_f32_16x16x32_bf16 v[12:15], v[40:43], v[76:79], v[12:15]\n"
    " v_mfma_f32_16x16x32_bf16 v[16:19], v[44:47], v[76:79], v[16:19]\n"
    " v_mfma_f32_16x16x32_bf16 v[20:23], v[48:51], v[76:79], v[20:23]\n"
    " v_mfma_f32_16x16x32_bf16 v[24:27], v[52:55], v[76:79], v[24:27]\n"
    " v_mfma_f32_16x16x32_bf16 v[28:31], v[56:59], v[76:79], v[28:31]\n"
    " v_mfma_f32_16x16x32_bf16 v[32:35], v[60:63], v[76:79], v[32:35]\n"
    " v_mfma_f32_16x16x32_bf16 v[36:39], v[64:67], v[76:79], v[36:39]\n"
    " ds_read_b128 v[76:79], v3 offset:3456\n"
    " ds_read_b128 v[64:67], v2 offset:896\n"
    " buffer_load_dwordx4 v[156:159], v1, s[12:15], s87 offen offset:3072 sc1\n"
    " v_mfma_f32_16x16x32_bf16 v[16:19], v[40:43], v[80:83], v[16:19]\n"
    " v_mfma_f32_16x16x32_bf16 v[20:23], v[44:47], v[80:83], v[20:23]\n"
    " v_mfma_f32_16x16x32_bf16 v[24:27], v[48:51], v[80:83], v[24:27]\n"
    " v_mfma_f32_16x16x32_bf16 v[28:31], v[52:55], v[80:83], v[28:31]\n"
    " v_mfma_f32_16x16x32_bf16 v[32:35], v[56:59], v[80:83], v[32:35]\n"
    " v_mfma_f32_16x16x32_bf16 v[36:39], v[60:63], v[80:83], v[36:39]\n"
    " ds_read_b128 v[80:83], v3 offset:2944\n"
    " ds_read_b128 v[60:63], v2 offset:1408\n"
    " s_add_u32 s87, s87, 4096\n"
    " buffer_load_dwordx4 v[160:163], v1, s[12:15], s87 offen offset:0 sc1\n"
    " v_mfma_f32_16x16x32_bf16 v[20:23], v[40:43], v[84:87], v[20:23]\n"
    " v_mfma_f32_16x16x32_bf16 v[24:27], v[44:47], v[84:87], v[24:27]\n"
    " v_mfma_f32_16x16x32_bf16 v[28:31], v[48:51], v[84:87], v[28:31]\n"
    " v_mfma_f32_16x16x32_bf16 v[32:35], v[52:55], v[84:87], v[32:35]\n"
    " v_mfma_f32_16x16x32_bf16 v[36:39], v[56:59], v[84:87], v[36:39]\n"
    " ds_read_b128 v[84:87], v3 offset:2432\n"
    " ds_read_b128 v[56:59], v2 offset:1920\n"
    " v_mfma_f32_16x16x32_bf16 v[24:27], v[40:43], v[88:91], v[24:27]\n"
    " v_mfma_f32_16x16x32_bf16 v[28:31], v[44:47], v[88:91], v[28:31]\n"
    " buffer_load_dwordx4 v[164:167], v1, s[12:15], s87 offen offset:1024 sc1\n"
    " v_mfma_f32_16x16x32_bf16 v[32:35], v[48:51], v[88:91], v[32:35]\n"
    " v_mfma_f32_16x16x32_bf16 v[36:39], v[52:55], v[88:91], v[36:39]\n"
    " ds_read_b128 v[88:91], v3 offset:1920\n"
    " ds_read_b128 v[52:55], v2 offset:2432\n"
    " v_mfma_f32_16x16x32_bf16 v[28:31], v[40:43], v[92:95], v[28:31]\n"
    " v_mfma_f32_16x16x32_bf16 v[32:35], v[44:47], v[92:95], v[32:35]\n"
    " v_mfma_f32_16x16x32_bf16 v[36:39], v[48:51], v[92:95], v[36:39]\n"
    " ds_read_b128 v[92:95], v3 offset:1408\n"
    " ds_read_b128 v[48:51], v2 offset:2944\n"
    " v_mfma_f32_16x16x32_bf16 v[32:35], v[40:43], v[96:99], v[32:35]\n"
    " v_mfma_f32_16x16x32_bf16 v[36:39], v[44:47], v[96:99], v[36:39]\n"
    " ds_read_b128 v[96:99], v3 offset:896\n"
    " ds_read_b128 v[44:47], v2 offset:3456\n"
    " v_mfma_f32_16x16x32_bf16 v[36:39], v[40:43], v[100:103], v[36:39]\n"
    " ds_read_b128 v[100:103], v3 offset:384\n"
    " ds_read_b128 v[40:43], v2 offset:3968\n"
    " s_waitcnt lgkmcnt(14)\n"
    " v_mfma_f32_16x16x32_bf16 v[8:11], v[68:71], v[72:75], v[8:11]\n"
    " s_waitcnt lgkmcnt(12)\n"
    " v_mfma_f32_16x16x32_bf16 v[12:15], v[64:67], v[72:75], v[12:15]\n"
    " buffer_load_dwordx4 v[168:171], v1, s[12:15], s87 offen offset:2048 sc1\n"
    " v_mfma_f32_16x16x32_bf16 v[12:15], v[68:71], v[76:79], v[12:15]\n"
    " s_waitcnt lgkmcnt(10)\n"
    " v_mfma_f32_16x16x32_bf16 v[16:19], v[60:63], v[72:75], v[16:19]\n"
    " v_mfma_f32_16x16x32_bf16 v[16:19], v[64:67], v[76:79], v[16:19]\n"
    " v_mfma_f32_16x16x32_bf16 v[16:19], v[68:71], v[80:83], v[16:19]\n"
    " s_waitcnt lgkmcnt(8)\n"
    " v_mfma_f32_16x16x32_bf16 v[20:23], v[56:59], v[72:75], v[20:23]\n"
    " buffer_load_dwordx4 v[172:175], v1, s[12:15], s87 offen offset:3072 sc1\n"
    " v_mfma_f32_16x16x32_bf16 v[20:23], v[60:63], v[76:79], v[20:23]\n"
    " v_mfma_f32_16x16x32_bf16 v[20:23], v[64:67], v[80:83], v[20:23]\n"
    " v_mfma_f32_16x16x32_bf16 v[20:23], v[68:71], v[84:87], v[20:23]\n"
    " ds_write_b128 v104, v[8:11] offset:0\n"
    " s_waitcnt lgkmcnt(7)\n"
    " v_mfma_f32_16x16x32_bf16 v[24:27], v[52:55], v[72:75], v[24:27]\n"
    " v_mfma_f32_16x16x32_bf16 v[24:27], v[56:59], v[76:79], v[24:27]\n"
    " ds_write_b128 v104, v[12:15] offset:1024\n"
    " v_mfma_f32_16x16x32_bf16 v[24:27], v[60:63], v[80:83], v[24:27]\n"
    " s_add_u32 s87, s87, 4096\n"
    " buffer_load_dwordx4 v[176:179], v1, s[12:15], s87 offen offset:0 sc1\n"
    " v_mfma_f32_16x16x32_bf16 v[24:27], v[64:67], v[84:87], v[24:27]\n"
    " v_mfma_f32_16x16x32_bf16 v[24:27], v[68:71], v[88:91], v[24:27]\n"
    " ds_write_b128 v104, v[16:19] offset:2048\n"
    " s_waitcnt lgkmcnt(7)\n"
    " v_mfma_f32_16x16x32_bf16 v[28:31], v[48:51], v[72:75], v[28:31]\n"
    " v_mfma_f32_16x16x32_bf16 v[28:31], v[52:55], v[76:79], v[28:31]\n"
    " v_mfma_f32_16x16x32_bf16 v[28:31], v[56:59], v[80:83], v[28:31]\n"
    " buffer_load_dwordx4 v[180:183], v1, s[12:15], s87 offen offset:1024 sc1\n"
    " v_mfma_f32_16x16x32_bf16 v[28:31], v[60:63], v[84:87], v[28:31]\n"
    " ds_write_b128 v104, v[20:23] offset:3072\n"
    " v_mfma_f32_16x16x32_bf16 v[28:31], v[64:67], v[88:91], v[28:31]\n"
    " v_mfma_f32_16x16x32_bf16 v[28:31], v[68:71], v[92:95], v[28:31]\n"
    " s_waitcnt lgkmcnt(6)\n"
    " v_mfma_f32_16x16x32_bf16 v[32:35], v[44:47], v[72:75], v[32:35]\n"
    " v_mfma_f32_16x16x32_bf16 v[32:35], v[48:51], v[76:79], v[32:35]\n"
    " v_mfma_f32_16x16x32_bf16 v[32:35], v[52:55], v[80:83], v[32:35]\n"
    " ds_write_b128 v104, v[24:27] offset:4096\n"
    " buffer_load_dwordx4 v[184:187], v1, s[12:15], s87 offen offset:2048 sc1\n"
    " v_mfma_f32_16x16x32_bf16 v[32:35], v[56:59], v[84:87], v[32:35]\n"
    " v_mfma_f32_16x16x32_bf16 v[32:35], v[60:63], v[88:91], v[32:35]\n"
    " v_mfma_f32_16x16x32_bf16 v[32:35], v[64:67], v[92:95], v[32:35]\n"
    " v_mfma_f32_16x16x32_bf16 v[32:35], v[68:71], v[96:99], v[32:35]\n"
    " s_waitcnt lgkmcnt(5)\n"
    " v_mfma_f32_16x16x32_bf16 v[36:39], v[40:43], v[72:75], v[36:39]\n"
    " buffer_load_dwordx4 v[188:191], v1, s[12:15], s87 offen offset:3072 sc1\n"
    " v_mfma_f32_16x16x32_bf16 v[36:39], v[44:47], v[76:79], v[36:39]\n"
    " ds_write_b128 v104, v[28:31] offset:5120\n"
    " v_mfma_f32_16x16x32_bf16 v[36:39], v[48:51], v[80:83], v[36:39]\n"
    " v_mfma_f32_16x16x32_bf16 v[36:39], v[52:55], v[84:87], v[36:39]\n"
    " v_mfma_f32_16x16x32_bf16 v[36:39], v[56:59], v[88:91], v[36:39]\n"
    " v_mfma_f32_16x16x32_bf16 v[36:39], v[60:63], v[92:95], v[36:39]\n"
    " v_mfma_f32_16x16x32_bf16 v[36:39], v[64:67], v[96:99], v[36:39]\n"
    " v_mfma_f32_16x16x32_bf16 v[36:39], v[68:71], v[100:103], v[36:39]\n"
    " ds_write_b128 v104, v[32:35] offset:6144\n"
    " s_nop 6\n"
    " ds_write_b128 v104, v[36:39] offset:7168\n"
    " v_mov_b32 v106, 141824\n"
    " v_mov_b32 v107, 1\n"
    " s_mov_b64 exec, 1\n"
    " ds_add_u32 v106, v107 offset:12\n"
    " s_mov_b64 exec, -1\n"
    " s_lshl_b32 s60, s48, 7\n"
    " s_add_u32 s60, s60, 200704\n"
    " v_and_b32 v5, 7, v0\n"
    " v_lshl_add_u32 v5, v5, 12, s60\n"
    " s_cmp_lg_u32 s44, 0\n"
    " s_cbranch_scc1 .Lnox\n"
    ".Lfp_10:\n"
    " buffer_load_dwordx2 v[106:107], v5, s[12:15], 0 offen sc1\n"
    " s_waitcnt vmcnt(0)\n"
    " v_xor_b32 v106, s50, v106\n"
    " v_xor_b32 v107, s51, v107\n"
    " v_or_b32 v106, v106, v107\n"
    " v_cmp_ne_u32 vcc, 0, v106\n"
    " s_cbranch_vccz .Lsp_11\n"
    " s_sleep 4\n"
    " s_sub_u32 s57, s57, 1\n"
    " s_cbranch_scc0 .Lfp_10\n"
    " s_mov_b32 s57, 0\n"
    ".Lsp_11:\n"
    ".Lnox:\n"
    " s_add_u32 s87, s87, 4096\n"
    " buffer_load_dwordx4 v[192:195], v1, s[12:15], s87 offen offset:0 sc1\n"
    " buffer_load_dwordx4 v[196:199], v1, s[12:15], s87 offen offset:1024 sc1\n"
    " buffer_load_dwordx4 v[200:203], v1, s[12:15], s87 offen offset:2048 sc1\n"
    " buffer_load_dwordx4 v[204:207], v1, s[12:15], s87 offen offset:3072 sc1\n"
    " s_add_u32 s87, s87, 4096\n"
    " buffer_load_dwordx4 v[208:211], v1, s[12:15], s87 offen offset:0 sc1\n"
    " buffer_load_dwordx4 v[212:215], v1, s[12:15], s87 offen offset:1024 sc1\n"
    " buffer_load_dwordx4 v[216:219], v1, s[12:15], s87 offen offset:2048 sc1\n"
    " buffer_load_dwordx4 v[220:223], v1, s[12:15], s87 offen offset:3072 sc1\n"
    " s_waitcnt lgkmcnt(0)\n"
    " s_branch .Lpre\n"
    ".Lhelp2:\n"
    " s_barrier\n"
    " s_setprio 2\n"
    " s_waitcnt lgkmcnt(0)\n"
    " s_add_u32 s50, s50, 1\n"
    " s_xor_b32 s51, s50, 0x5bd1e995\n"
    " s_mov_b32 s90, 0x80008000\n"
    " s_bitcmp1_b32 s50, 0\n"
    " s_cselect_b32 s88, 0xffff, s53\n"
    " s_and_b32 s88, s88, s90\n"
    " s_xor_b32 s89, s88, s90\n"
    " s_cmp_eq_u32 s44, 6\n"
    " s_cbranch_scc1 .Lrole6\n"
    " s_lshl_b32 s67, s70, 16\n"
    " s_lshl_b32 s68, s71, 13\n"
    " s_add_u32 s67, s67, s68\n"
    " s_add_u32 s67, s67, 5242880\n"
    " v_add_u32 v19, s67, v1\n"
    " v_add_u32 v11, 4096, v19\n"
    " buffer_store_dwordx4 v[12:15], v19, s[12:15], 0 offen offset:0 sc1\n"
    " buffer_store_dwordx4 v[12:15], v19, s[12:15], 0 offen offset:1024 sc1\n"
    " buffer_store_dwordx4 v[12:15], v19, s[12:15], 0 offen offset:2048 sc1\n"
    " buffer_store_dwordx4 v[12:15], v19, s[12:15], 0 offen offset:3072 sc1\n"
    " buffer_store_dwordx4 v[12:15], v11, s[12:15], 0 offen offset:0 sc1\n"
    " buffer_store_dwordx4 v[12:15], v11, s[12:15], 0 offen offset:1024 sc1\n"
    " buffer_store_dwordx4 v[12:15], v11, s[12:15], 0 offen offset:2048 sc1\n"
    " buffer_store_dwordx4 v[12:15], v11, s[12:15], 0 offen offset:3072 sc1\n"
    " s_lshl_b32 s63, s71, 18\n"
    " s_lshl_b32 s64, s70, 7\n"
    " s_add_u32 s63, s63, s64\n"
    " s_add_u32 s63, s63, 3145728\n"
    " v_lshl_add_u32 v18, v7, 13, v9\n"
    " v_add_u32 v18, s63, v18\n"
    " v_add_u32 v24, 0, v18\n"
    " v_add_u32 v25, 4096, v18\n"
    " v_add_u32 v26, 65536, v18\n"
    " v_add_u32 v27, 69632, v18\n"
    " v_add_u32 v28, 131072, v18\n"
    " v_add_u32 v29, 135168, v18\n"
    " v_add_u32 v30, 196608, v18\n"
    " v_add_u32 v31, 200704, v18\n"
    " v_mul_u32_u24 v5, 1040, v8\n"
    " v_lshl_add_u32 v5, v7, 2, v5\n"
    " v_add_u32 v5, 133504, v5\n"
    " s_mov_b32 s65, 0x05040100\n"
    " s_mov_b32 s66, 0x07060302\n"
    " s_waitcnt vmcnt(8)\n"
    " ds_read2_b32 v[32:33], v5 offset0:0 offset1:32\n"
    " ds_read2_b32 v[34:35], v5 offset0:64 offset1:96\n"
    " ds_read2_b32 v[36:37], v5 offset0:128 offset1:160\n"
    " ds_read2_b32 v[38:39], v5 offset0:192 offset1:224\n"
    " ds_read2_b32 v[40:41], v5 offset0:8 offset1:40\n"
    " ds_read2_b32 v[42:43], v5 offset0:72 offset1:104\n"
    " ds_read2_b32 v[44:45], v5 offset0:136 offset1:168\n"
    " ds_read2_b32 v[46:47], v5 offset0:200 offset1:232\n"
    " ds_read2_b32 v[48:49], v5 offset0:16 offset1:48\n"
    " ds_read2_b32 v[50:51], v5 offset0:80 offset1:112\n"
    " ds_read2_b32 v[52:53], v5 offset0:144 offset1:176\n"
    " ds_read2_b32 v[54:55], v5 offset0:208 offset1:240\n"
    " ds_read2_b32 v[56:57], v5 offset0:24 offset1:56\n"
    " ds_read2_b32 v[58:59], v5 offset0:88 offset1:120\n"
    " ds_read2_b32 v[60:61], v5 offset0:152 offset1:184\n"
    " ds_read2_b32 v[62:63], v5 offset0:216 offset1:248\n"
    " s_waitcnt lgkmcnt(15)\n"
    " v_perm_b32 v64, v33, v32, s65\n"
    " s_waitcnt lgkmcnt(14)\n"
    " v_perm_b32 v65, v35, v34, s65\n"
    " s_waitcnt lgkmcnt(13)\n"
    " v_perm_b32 v66, v37, v36, s65\n"
    " s_waitcnt lgkmcnt(12)\n"
    " v_perm_b32 v67, v39, v38, s65\n"
    " v_perm_b32 v68, v33, v32, s66\n"
    " v_perm_b32 v69, v35, v34, s66\n"
    " v_perm_b32 v70, v37, v36, s66\n"
    " v_perm_b32 v71, v39, v38, s66\n"
    " s_waitcnt lgkmcnt(11)\n"
    " v_perm_b32 v72, v41, v40, s65\n"
    " s_waitcnt lgkmcnt(10)\n"
    " v_perm_b32 v73, v43, v42, s65\n"
    " s_waitcnt lgkmcnt(9)\n"
    " v_perm_b32 v74, v45, v44, s65\n"
    " s_waitcnt lgkmcnt(8)\n"
    " v_perm_b32 v75, v47, v46, s65\n"
    " v_perm_b32 v76, v41, v40, s66\n"
    " v_perm_b32 v77, v43, v42, s66\n"
    " v_perm_b32 v78, v45, v44, s66\n"
    " v_perm_b32 v79, v47, v46, s66\n"
    " s_waitcnt lgkmcnt(7)\n"
    " v_perm_b32 v80, v49, v48, s65\n"
    " s_waitcnt lgkmcnt(6)\n"
    " v_perm_b32 v81, v51, v50, s65\n"
    " s_waitcnt lgkmcnt(5)\n"
    " v_perm_b32 v82, v53, v52, s65\n"
    " s_waitcnt lgkmcnt(4)\n"
    " v_perm_b32 v83, v55, v54, s65\n"
    " v_perm_b32 v84, v49, v48, s66\n"
    " v_perm_b32 v85, v51, v50, s66\n"
    " v_perm_b32 v86, v53, v52, s66\n"
    " v_perm_b32 v87, v55, v54, s66\n"
    " s_waitcnt lgkmcnt(3)\n"
    " v_perm_b32 v88, v57, v56, s65\n"
    " s_waitcnt lgkmcnt(2)\n"
    " v_perm_b32 v89, v59, v58, s65\n"
    " s_waitcnt lgkmcnt(1)\n"
    " v_perm_b32 v90, v61, v60, s65\n"
    " s_waitcnt lgkmcnt(0)\n"
    " v_perm_b32 v91, v63, v62, s65\n"
    " v_perm_b32 v92, v57, v56, s66\n"
    " v_perm_b32 v93, v59, v58, s66\n"
    " v_perm_b32 v94, v61, v60, s66\n"
    " v_perm_b32 v95, v63, v62, s66\n"
    " buffer_store_dwordx4 v[64:67], v24, s[12:15], 0 offen sc1\n"
    " buffer_store_dwordx4 v[68:71], v25, s[12:15], 0 offen sc1\n"
    " buffer_store_dwordx4 v[72:75], v26, s[12:15], 0 offen sc1\n"
    " buffer_store_dwordx4 v[76:79], v27, s[12:15], 0 offen sc1\n"
    " buffer_store_dwordx4 v[80:83], v28, s[12:15], 0 offen sc1\n"
    " buffer_store_dwordx4 v[84:87], v29, s[12:15], 0 offen sc1\n"
    " buffer_store_dwordx4 v[88:91], v30, s[12:15], 0 offen sc1\n"
    " buffer_store_dwordx4 v[92:95], v31, s[12:15], 0 offen sc1\n"
    " s_lshl_b32 s60, s71, 5\n"
    " s_add_u32 s60, s60, s70\n"
    " s_lshl_b32 s60, s60, 7\n"
    " s_add_u32 s60, s60, 200704\n"
    " s_waitcnt vmcnt(0)\n"
    " v_mov_b32 v22, s50\n"
    " v_mov_b32 v23, s51\n"
    " v_lshrrev_b32 v24, 1, v1\n"
    " s_mov_b64 exec, 0xffff\n"
    " buffer_store_dwordx2 v[22:23], v24, s[12:15], s60 offen sc1\n"
    " s_mov_b64 exec, -1\n"
    " s_branch .Lhjoin\n"
    ".Lrole45:\n"
    " v_lshlrev_b32 v18, 4, v16\n"
    " v_lshl_add_u32 v5, v17, 10, v18\n"
    " s_lshr_b32 s60, s2, 2\n"
    " s_lshl_b32 s60, s60, 15\n"
    " s_and_b32 s63, s2, 3\n"
    " s_lshl_b32 s63, s63, 8\n"
    " s_add_u32 s60, s60, s63\n"
    " s_mov_b32 s61, 4096\n"
    " s_mov_b64 s[24:25], s[32:33]\n"
    " s_mov_b32 s26, s34\n"
    " s_cmp_eq_u32 s44, 5\n"
    " s_cbranch_scc1 .Lwissue\n"
    " v_mul_u32_u24 v5, 3584, v17\n"
    " v_add_u32 v5, v5, v18\n"
    " s_and_b32 s60, s2, 15\n"
    " s_mul_i32 s60, s60, 114688\n"
    " s_lshr_b32 s63, s2, 4\n"
    " s_lshl_b32 s63, s63, 8\n"
    " s_add_u32 s60, s60, s63\n"
    " s_mov_b32 s61, 14336\n"
    " s_mov_b64 s[24:25], s[28:29]\n"
    ".Lwissue:\n"
    " s_waitcnt lgkmcnt(0)\n"
    " s_add_u32 s50, s50, 1\n"
    " s_xor_b32 s51, s50, 0x5bd1e995\n"
    " s_mov_b32 s90, 0x80008000\n"
    " s_bitcmp1_b32 s50, 0\n"
    " s_cselect_b32 s88, 0xffff, s53\n"
    " s_and_b32 s88, s88, s90\n"
    " s_xor_b32 s89, s88, s90\n"
    " s_cmp_eq_u32 s44, 4\n"
    " s_cbranch_scc1 .Lwgo\n"
    " s_barrier\n"
    " s_cmp_eq_u32 s44, 4\n"
    " s_cbranch_scc1 .Lwgo\n"
    ".Lwgo:\n"
    " s_cmp_ge_u32 s2, 224\n"
    " s_cbranch_scc1 .Lwskip\n"
    " s_sleep 6\n"
    " s_mov_b32 m0, s69\n"
    " s_nop 0\n"
    " buffer_load_dwordx4 v5, s[24:27], s60 offen nt lds\n"
    " s_add_u32 m0, m0, 1040\n"
    " s_add_u32 s60, s60, s61\n"
    " buffer_load_dwordx4 v5, s[24:27], s60 offen nt lds\n"
    " s_add_u32 m0, m0, 1040\n"
    " s_add_u32 s60, s60, s61\n"
    " buffer_load_dwordx4 v5, s[24:27], s60 offen nt lds\n"
    " s_add_u32 m0, m0, 1040\n"
    " s_add_u32 s60, s60, s61\n"
    " buffer_load_dwordx4 v5, s[24:27], s60 offen nt lds\n"
    " s_add_u32 m0, m0, 1040\n"
    " s_add_u32 s60, s60, s61\n"
    " buffer_load_dwordx4 v5, s[24:27], s60 offen nt lds\n"
    " s_add_u32 m0, m0, 1040\n"
    " s_add_u32 s60, s60, s61\n"
    " buffer_load_dwordx4 v5, s[24:27], s60 offen nt lds\n"
    " s_add_u32 m0, m0, 1040\n"
    " s_add_u32 s60, s60, s61\n"
    " buffer_load_dwordx4 v5, s[24:27], s60 offen nt lds\n"
    " s_add_u32 m0, m0, 1040\n"
    " s_add_u32 s60, s60, s61\n"
    " buffer_load_dwordx4 v5, s[24:27], s60 offen nt lds\n"
    ".Lwskip:\n"
    " s_cmp_lg_u32 s44, 4\n"
    " s_cbranch_scc1 .Lwnob\n"
    " s_barrier\n"
    ".Lwnob:\n"
    " s_cmp_ge_u32 s2, 224\n"
    " s_cbranch_scc1 .Lwpoll\n"
    " v_lshrrev_b32 v20, 2, v16\n"
    " v_and_b32 v21, 3, v16\n"
    " v_lshlrev_b32 v21, 3, v21\n"
    " v_lshl_add_u32 v20, v20, 8, v21\n"
    " s_mov_b32 s60, 1040\n"
    " s_mov_b32 s61, 4160\n"
    " s_and_b32 s63, s2, 3\n"
    " s_mul_i32 s63, s63, 112\n"
    " s_lshr_b32 s64, s2, 2\n"
    " s_add_u32 s63, s63, s64\n"
    " s_lshl_b32 s63, s63, 12\n"
    " s_add_u32 s63, s63, 9175040\n"
    " s_mov_b32 s65, 2048\n"
    " s_mov_b32 s66, 229376\n"
    " s_cmp_eq_u32 s44, 5\n"
    " s_cbranch_scc1 .Lr45go\n"
    " s_mov_b32 s60, 2080\n"
    " s_mov_b32 s61, 1040\n"
    " s_lshr_b32 s63, s2, 4\n"
    " s_lshl_b32 s63, s63, 6\n"
    " s_and_b32 s64, s2, 15\n"
    " s_add_u32 s63, s63, s64\n"
    " s_lshl_b32 s63, s63, 11\n"
    " s_add_u32 s63, s63, 7340032\n"
    " s_mov_b32 s65, 32768\n"
    " s_mov_b32 s66, 65536\n"
    ".Lr45go:\n"
    " v_mad_u32_u24 v20, v17, s60, v20\n"
    " v_add_u32 v20, s69, v20\n"
    " v_add_u32 v21, s61, v20\n"
    " v_add_u32 v24, s63, v1\n"
    " v_add_u32 v25, s65, v24\n"
    " v_add_u32 v26, s66, v24\n"
    " v_add_u32 v27, s65, v26\n"
    " s_waitcnt vmcnt(0)\n"
    " ds_read_b64_tr_b16 v[32:33], v20 offset:0\n"
    " ds_read_b64_tr_b16 v[34:35], v21 offset:0\n"
    " ds_read_b64_tr_b16 v[36:37], v20 offset:32\n"
    " ds_read_b64_tr_b16 v[38:39], v21 offset:32\n"
    " ds_read_b64_tr_b16 v[40:41], v20 offset:64\n"
    " ds_read_b64_tr_b16 v[42:43], v21 offset:64\n"
    " ds_read_b64_tr_b16 v[44:45], v20 offset:96\n"
    " ds_read_b64_tr_b16 v[46:47], v21 offset:96\n"
    " ds_read_b64_tr_b16 v[48:49], v20 offset:128\n"
    " ds_read_b64_tr_b16 v[50:51], v21 offset:128\n"
    " ds_read_b64_tr_b16 v[52:53], v20 offset:160\n"
    " ds_read_b64_tr_b16 v[54:55], v21 offset:160\n"
    " ds_read_b64_tr_b16 v[56:57], v20 offset:192\n"
    " ds_read_b64_tr_b16 v[58:59], v21 offset:192\n"
    " ds_read_b64_tr_b16 v[60:61], v20 offset:224\n"
    " ds_read_b64_tr_b16 v[62:63], v21 offset:224\n"
    " s_cmp_eq_u32 s44, 5\n"
    " s_cselect_b32 s67, s88, 0\n"
    " s_cselect_b32 s68, s89, 0\n"
    " s_waitcnt lgkmcnt(15)\n"
    " v_xor_b32 v32, s67, v32\n"
    " v_xor_b32 v33, s68, v33\n"
    " s_waitcnt lgkmcnt(14)\n"
    " v_xor_b32 v34, s68, v34\n"
    " v_xor_b32 v35, s67, v35\n"
    " buffer_store_dwordx4 v[32:35], v24, s[12:15], 0 offen offset:0 sc1\n"
    " s_waitcnt lgkmcnt(13)\n"
    " v_xor_b32 v36, s67, v36\n"
    " v_xor_b32 v37, s68, v37\n"
    " s_waitcnt lgkmcnt(12)\n"
    " v_xor_b32 v38, s68, v38\n"
    " v_xor_b32 v39, s67, v39\n"
    " buffer_store_dwordx4 v[36:39], v24, s[12:15], 0 offen offset:1024 sc1\n"
    " s_waitcnt lgkmcnt(11)\n"
    " v_xor_b32 v40, s67, v40\n"
    " v_xor_b32 v41, s68, v41\n"
    " s_waitcnt lgkmcnt(10)\n"
    " v_xor_b32 v42, s68, v42\n"
    " v_xor_b32 v43, s67, v43\n"
    " buffer_store_dwordx4 v[40:43], v25, s[12:15], 0 offen offset:0 sc1\n"
    " s_waitcnt lgkmcnt(9)\n"
    " v_xor_b32 v44, s67, v44\n"
    " v_xor_b32 v45, s68, v45\n"
    " s_waitcnt lgkmcnt(8)\n"
    " v_xor_b32 v46, s68, v46\n"
    " v_xor_b32 v47, s67, v47\n"
    " buffer_store_dwordx4 v[44:47], v25, s[12:15], 0 offen offset:1024 sc1\n"
    " s_waitcnt lgkmcnt(7)\n"
    " v_xor_b32 v48, s67, v48\n"
    " v_xor_b32 v49, s68, v49\n"
    " s_waitcnt lgkmcnt(6)\n"
    " v_xor_b32 v50, s68, v50\n"
    " v_xor_b32 v51, s67, v51\n"
    " buffer_store_dwordx4 v[48:51], v26, s[12:15], 0 offen offset:0 sc1\n"
    " s_waitcnt lgkmcnt(5)\n"
    " v_xor_b32 v52, s67, v52\n"
    " v_xor_b32 v53, s68, v53\n"
    " s_waitcnt lgkmcnt(4)\n"
    " v_xor_b32 v54, s68, v54\n"
    " v_xor_b32 v55, s67, v55\n"
    " buffer_store_dwordx4 v[52:55], v26, s[12:15], 0 offen offset:1024 sc1\n"
    " s_waitcnt lgkmcnt(3)\n"
    " v_xor_b32 v56, s67, v56\n"
    " v_xor_b32 v57, s68, v57\n"
    " s_waitcnt lgkmcnt(2)\n"
    " v_xor_b32 v58, s68, v58\n"
    " v_xor_b32 v59, s67, v59\n"
    " buffer_store_dwordx4 v[56:59], v27, s[12:15], 0 offen offset:0 sc1\n"
    " s_waitcnt lgkmcnt(1)\n"
    " v_xor_b32 v60, s67, v60\n"
    " v_xor_b32 v61, s68, v61\n"
    " s_waitcnt lgkmcnt(0)\n"
    " v_xor_b32 v62, s68, v62\n"
    " v_xor_b32 v63, s67, v63\n"
    " buffer_store_dwordx4 v[60:63], v27, s[12:15], 0 offen offset:1024 sc1\n"
    " s_cmp_eq_u32 s44, 5\n"
    " s_cbranch_scc1 .Lwflag\n"
    " v_mov_b32 v28, 0x3f803f80\n"
    " v_mov_b32 v29, 0x3f803f80\n"
    " v_mov_b32 v30, 0x3f803f80\n"
    " v_mov_b32 v31, 0x3f803f80\n"
    " s_nop 1\n"
    " v_mfma_f32_16x16x32_bf16 v[64:67], v[28:31], v[32:35], 0\n"
    " v_mfma_f32_16x16x32_bf16 v[68:71], v[28:31], v[36:39], 0\n"
    " v_mfma_f32_16x16x32_bf16 v[72:75], v[28:31], v[40:43], 0\n"
    " v_mfma_f32_16x16x32_bf16 v[76:79], v[28:31], v[44:47], 0\n"
    " v_mfma_f32_16x16x32_bf16 v[80:83], v[28:31], v[48:51], 0\n"
    " v_mfma_f32_16x16x32_bf16 v[84:87], v[28:31], v[52:55], 0\n"
    " v_mfma_f32_16x16x32_bf16 v[88:91], v[28:31], v[56:59], 0\n"
    " v_mfma_f32_16x16x32_bf16 v[92:95], v[28:31], v[60:63], 0\n"
    " v_cmp_eq_u32 s[80:81], 1, v17\n"
    " v_cmp_eq_u32 s[82:83], 2, v17\n"
    " v_cmp_eq_u32 s[84:85], 3, v17\n"
    " s_lshl_b32 s60, s2, 9\n"
    " s_add_u32 s60, s60, 774144\n"
    " v_lshl_add_u32 v5, v6, 2, s60\n"
    " v_cndmask_b32 v64, v64, v68, s[80:81]\n"
    " v_cndmask_b32 v64, v64, v72, s[82:83]\n"
    " v_cndmask_b32 v64, v64, v76, s[84:85]\n"
    " buffer_store_dword v64, v5, s[12:15], 0 offen offset:0 sc1\n"
    " v_cndmask_b32 v80, v80, v84, s[80:81]\n"
    " v_cndmask_b32 v80, v80, v88, s[82:83]\n"
    " v_cndmask_b32 v80, v80, v92, s[84:85]\n"
    " buffer_store_dword v80, v5, s[12:15], 0 offen offset:256 sc1\n"
    ".Lwflag:\n"
    " s_lshl_b32 s60, s2, 7\n"
    " s_lshl_b32 s61, s44, 15\n"
    " s_add_u32 s60, s60, s61\n"
    " s_add_u32 s60, s60, 4096\n"
    " s_waitcnt vmcnt(0)\n"
    " v_mov_b32 v22, s50\n"
    " v_mov_b32 v23, s51\n"
    " v_lshrrev_b32 v24, 1, v1\n"
    " s_mov_b64 exec, 0xffff\n"
    " buffer_store_dwordx2 v[22:23], v24, s[12:15], s60 offen sc1\n"
    " s_mov_b64 exec, -1\n"
    ".Lwpoll:\n"
    " s_cmp_eq_u32 s44, 5\n"
    " s_cbranch_scc1 .Lw5\n"
    " s_mul_i32 s60, s49, 7\n"
    " s_add_u32 s61, s60, 6\n"
    " s_lshr_b32 s60, s60, 2\n"
    " s_lshr_b32 s61, s61, 2\n"
    " v_add_u32 v5, s60, v17\n"
    " v_min_u32 v5, s61, v5\n"
    " v_lshl_add_u32 v5, v5, 4, v16\n"
    " v_lshlrev_b32 v5, 7, v5\n"
    " v_add_u32 v5, 135168, v5\n"
    " v_mov_b32 v20, 141824\n"
    ".Llw_12:\n"
    " s_sleep 2\n"
    " ds_read_b32 v21, v20 offset:0\n"
    " s_waitcnt lgkmcnt(0)\n"
    " v_cmp_gt_u32 vcc, 4, v21\n"
    " s_cbranch_vccnz .Llw_12\n"
    " s_waitcnt vmcnt(0)\n"
    ".Lfp_13:\n"
    " buffer_load_dwordx2 v[22:23], v5, s[12:15], 0 offen sc1\n"
    " s_waitcnt vmcnt(0)\n"
    " v_xor_b32 v22, s50, v22\n"
    " v_xor_b32 v23, s51, v23\n"
    " v_or_b32 v22, v22, v23\n"
    " v_cmp_ne_u32 vcc, 0, v22\n"
    " s_cbranch_vccz .Lsp_14\n"
    " s_sleep 4\n"
    " s_sub_u32 s57, s57, 1\n"
    " s_cbranch_scc0 .Lfp_13\n"
    " s_mov_b32 s57, 0\n"
    ".Lsp_14:\n"
    " v_mov_b32 v20, 141824\n"
    " v_mov_b32 v21, 1\n"
    " s_mov_b64 exec, 1\n"
    " ds_add_u32 v20, v21 offset:16\n"
    " s_mov_b64 exec, -1\n"
    ".Lw5:\n"
    " s_waitcnt vmcnt(0)\n"
    " v_mov_b32 v20, 141824\n"
    ".Llw_15:\n"
    " s_sleep 2\n"
    " ds_read_b32 v21, v20 offset:16\n"
    " s_waitcnt lgkmcnt(0)\n"
    " v_cmp_gt_u32 vcc, 1, v21\n"
    " s_cbranch_vccnz .Llw_15\n"
    " v_mov_b32 v20, 141824\n"
    ".Llw_16:\n"
    " s_sleep 2\n"
    " ds_read_b32 v21, v20 offset:4\n"
    " s_waitcnt lgkmcnt(0)\n"
    " v_cmp_gt_u32 vcc, 4, v21\n"
    " s_cbranch_vccnz .Llw_16\n"
    " buffer_load_dwordx4 v[128:131], v1, s[12:15], s87 offen offset:0 sc1\n"
    " s_sleep 1\n"
    " buffer_load_dwordx4 v[132:135], v1, s[12:15], s87 offen offset:1024 sc1\n"
    " s_sleep 1\n"
    " buffer_load_dwordx4 v[136:139], v1, s[12:15], s87 offen offset:2048 sc1\n"
    " s_sleep 1\n"
    " buffer_load_dwordx4 v[140:143], v1, s[12:15], s87 offen offset:3072 sc1\n"
    " s_sleep 1\n"
    " s_add_u32 s87, s87, 4096\n"
    " buffer_load_dwordx4 v[144:147], v1, s[12:15], s87 offen offset:0 sc1\n"
    " s_sleep 1\n"
    " buffer_load_dwordx4 v[148:151], v1, s[12:15], s87 offen offset:1024 sc1\n"
    " s_sleep 1\n"
    " buffer_load_dwordx4 v[152:155], v1, s[12:15], s87 offen offset:2048 sc1\n"
    " s_sleep 1\n"
    " buffer_load_dwordx4 v[156:159], v1, s[12:15], s87 offen offset:3072 sc1\n"
    " s_sleep 1\n"
    " s_add_u32 s87, s87, 4096\n"
    " buffer_load_dwordx4 v[160:163], v1, s[12:15], s87 offen offset:0 sc1\n"
    " s_sleep 1\n"
    " buffer_load_dwordx4 v[164:167], v1, s[12:15], s87 offen offset:1024 sc1\n"
    " s_sleep 1\n"
    " buffer_load_dwordx4 v[168:171], v1, s[12:15], s87 offen offset:2048 sc1\n"
    " s_sleep 1\n"
    " buffer_load_dwordx4 v[172:175], v1, s[12:15], s87 offen offset:3072 sc1\n"
    " s_sleep 1\n"
    " s_add_u32 s87, s87, 4096\n"
    " buffer_load_dwordx4 v[176:179], v1, s[12:15], s87 offen offset:0 sc1\n"
    " s_sleep 1\n"
    " buffer_load_dwordx4 v[180:183], v1, s[12:15], s87 offen offset:1024 sc1\n"
    " s_sleep 1\n"
    " buffer_load_dwordx4 v[184:187], v1, s[12:15], s87 offen offset:2048 sc1\n"
    " s_sleep 1\n"
    " buffer_load_dwordx4 v[188:191], v1, s[12:15], s87 offen offset:3072 sc1\n"
    " s_sleep 1\n"
    " s_add_u32 s87, s87, 4096\n"
    " buffer_load_dwordx4 v[192:195], v1, s[12:15], s87 offen offset:0 sc1\n"
    " s_sleep 1\n"
    " buffer_load_dwordx4 v[196:199], v1, s[12:15], s87 offen offset:1024 sc1\n"
    " s_sleep 1\n"
    " buffer_load_dwordx4 v[200:203], v1, s[12:15], s87 offen offset:2048 sc1\n"
    " s_sleep 1\n"
    " buffer_load_dwordx4 v[204:207], v1, s[12:15], s87 offen offset:3072 sc1\n"
    " s_sleep 1\n"
    " s_add_u32 s87, s87, 4096\n"
    " buffer_load_dwordx4 v[208:211], v1, s[12:15], s87 offen offset:0 sc1\n"
    " s_sleep 1\n"
    " buffer_load_dwordx4 v[212:215], v1, s[12:15], s87 offen offset:1024 sc1\n"
    " s_sleep 1\n"
    " buffer_load_dwordx4 v[216:219], v1, s[12:15], s87 offen offset:2048 sc1\n"
    " s_sleep 1\n"
    " buffer_load_dwordx4 v[220:223], v1, s[12:15], s87 offen offset:3072 sc1\n"
    " s_sleep 1\n"
    " s_branch .Lpre\n"
    ".Lrole6:\n"
    " v_add_u32 v5, 125184, v1\n"
    " v_mov_b32 v20, 0x3f803f80\n"
    " v_mov_b32 v64, 0\n"
    " v_mov_b32 v65, 0\n"
    " v_mov_b32 v66, 0\n"
    " v_mov_b32 v67, 0\n"
    " v_mov_b32 v68, 0\n"
    " v_mov_b32 v69, 0\n"
    " v_mov_b32 v70, 0\n"
    " v_mov_b32 v71, 0\n"
    " v_mov_b32 v72, 0\n"
    " v_mov_b32 v73, 0\n"
    " v_mov_b32 v74, 0\n"
    " v_mov_b32 v75, 0\n"
    " v_mov_b32 v76, 0\n"
    " v_mov_b32 v77, 0\n"
    " v_mov_b32 v78, 0\n"
    " v_mov_b32 v79, 0\n"
    " s_waitcnt vmcnt(0)\n"
    " ds_read_b128 v[32:35], v5 offset:0\n"
    " ds_read_b128 v[36:39], v5 offset:1040\n"
    " ds_read_b128 v[40:43], v5 offset:2080\n"
    " ds_read_b128 v[44:47], v5 offset:3120\n"
    " ds_read_b128 v[48:51], v5 offset:4160\n"
    " ds_read_b128 v[52:55], v5 offset:5200\n"
    " ds_read_b128 v[56:59], v5 offset:6240\n"
    " ds_read_b128 v[60:63], v5 offset:7280\n"
    " s_waitcnt lgkmcnt(7)\n"
    " v_dot2c_f32_bf16 v64, v20, v32\n"
    " v_dot2c_f32_bf16 v72, v32, v32\n"
    " s_waitcnt lgkmcnt(6)\n"
    " v_dot2c_f32_bf16 v65, v20, v36\n"
    " v_dot2c_f32_bf16 v73, v36, v36\n"
    " s_waitcnt lgkmcnt(5)\n"
    " v_dot2c_f32_bf16 v66, v20, v40\n"
    " v_dot2c_f32_bf16 v74, v40, v40\n"
    " s_waitcnt lgkmcnt(4)\n"
    " v_dot2c_f32_bf16 v67, v20, v44\n"
    " v_dot2c_f32_bf16 v75, v44, v44\n"
    " s_waitcnt lgkmcnt(3)\n"
    " v_dot2c_f32_bf16 v68, v20, v48\n"
    " v_dot2c_f32_bf16 v76, v48, v48\n"
    " s_waitcnt lgkmcnt(2)\n"
    " v_dot2c_f32_bf16 v69, v20, v52\n"
    " v_dot2c_f32_bf16 v77, v52, v52\n"
    " s_waitcnt lgkmcnt(1)\n"
    " v_dot2c_f32_bf16 v70, v20, v56\n"
    " v_dot2c_f32_bf16 v78, v56, v56\n"
    " s_waitcnt lgkmcnt(0)\n"
    " v_dot2c_f32_bf16 v71, v20, v60\n"
    " v_dot2c_f32_bf16 v79, v60, v60\n"
    " v_dot2c_f32_bf16 v64, v20, v33\n"
    " v_dot2c_f32_bf16 v72, v33, v33\n"
    " v_dot2c_f32_bf16 v65, v20, v37\n"
    " v_dot2c_f32_bf16 v73, v37, v37\n"
    " v_dot2c_f32_bf16 v66, v20, v41\n"
    " v_dot2c_f32_bf16 v74, v41, v41\n"
    " v_dot2c_f32_bf16 v67, v20, v45\n"
    " v_dot2c_f32_bf16 v75, v45, v45\n"
    " v_dot2c_f32_bf16 v68, v20, v49\n"
    " v_dot2c_f32_bf16 v76, v49, v49\n"
    " v_dot2c_f32_bf16 v69, v20, v53\n"
    " v_dot2c_f32_bf16 v77, v53, v53\n"
    " v_dot2c_f32_bf16 v70, v20, v57\n"
    " v_dot2c_f32_bf16 v78, v57, v57\n"
    " v_dot2c_f32_bf16 v71, v20, v61\n"
    " v_dot2c_f32_bf16 v79, v61, v61\n"
    " v_dot2c_f32_bf16 v64, v20, v34\n"
    " v_dot2c_f32_bf16 v72, v34, v34\n"
    " v_dot2c_f32_bf16 v65, v20, v38\n"
    " v_dot2c_f32_bf16 v73, v38, v38\n"
    " v_dot2c_f32_bf16 v66, v20, v42\n"
    " v_dot2c_f32_bf16 v74, v42, v42\n"
    " v_dot2c_f32_bf16 v67, v20, v46\n"
    " v_dot2c_f32_bf16 v75, v46, v46\n"
    " v_dot2c_f32_bf16 v68, v20, v50\n"
    " v_dot2c_f32_bf16 v76, v50, v50\n"
    " v_dot2c_f32_bf16 v69, v20, v54\n"
    " v_dot2c_f32_bf16 v77, v54, v54\n"
    " v_dot2c_f32_bf16 v70, v20, v58\n"
    " v_dot2c_f32_bf16 v78, v58, v58\n"
    " v_dot2c_f32_bf16 v71, v20, v62\n"
    " v_dot2c_f32_bf16 v79, v62, v62\n"
    " v_dot2c_f32_bf16 v64, v20, v35\n"
    " v_dot2c_f32_bf16 v72, v35, v35\n"
    " v_dot2c_f32_bf16 v65, v20, v39\n"
    " v_dot2c_f32_bf16 v73, v39, v39\n"
    " v_dot2c_f32_bf16 v66, v20, v43\n"
    " v_dot2c_f32_bf16 v74, v43, v43\n"
    " v_dot2c_f32_bf16 v67, v20, v47\n"
    " v_dot2c_f32_bf16 v75, v47, v47\n"
    " v_dot2c_f32_bf16 v68, v20, v51\n"
    " v_dot2c_f32_bf16 v76, v51, v51\n"
    " v_dot2c_f32_bf16 v69, v20, v55\n"
    " v_dot2c_f32_bf16 v77, v55, v55\n"
    " v_dot2c_f32_bf16 v70, v20, v59\n"
    " v_dot2c_f32_bf16 v78, v59, v59\n"
    " v_dot2c_f32_bf16 v71, v20, v63\n"
    " v_dot2c_f32_bf16 v79, v63, v63\n"
    " v_permlane32_swap_b32 v64, v68\n"
    " v_permlane32_swap_b32 v65, v69\n"
    " v_permlane32_swap_b32 v66, v70\n"
    " v_permlane32_swap_b32 v67, v71\n"
    " v_permlane32_swap_b32 v72, v76\n"
    " v_permlane32_swap_b32 v73, v77\n"
    " v_permlane32_swap_b32 v74, v78\n"
    " v_permlane32_swap_b32 v75, v79\n"
    " v_add_f32 v64, v64, v68\n"
    " v_add_f32 v65, v65, v69\n"
    " v_add_f32 v66, v66, v70\n"
    " v_add_f32 v67, v67, v71\n"
    " v_add_f32 v72, v72, v76\n"
    " v_add_f32 v73, v73, v77\n"
    " v_add_f32 v74, v74, v78\n"
    " v_add_f32 v75, v75, v79\n"
    " v_permlane16_swap_b32 v64, v66\n"
    " v_permlane16_swap_b32 v65, v67\n"
    " v_permlane16_swap_b32 v72, v74\n"
    " v_permlane16_swap_b32 v73, v75\n"
    " v_add_f32 v64, v64, v66\n"
    " v_add_f32 v65, v65, v67\n"
    " v_add_f32 v72, v72, v74\n"
    " v_add_f32 v73, v73, v75\n"
    " v_add_f32_dpp v64, v64, v64 quad_perm:[1,0,3,2] row_mask:0xf bank_mask:0xf\n"
    " v_add_f32_dpp v65, v65, v65 quad_perm:[1,0,3,2] row_mask:0xf bank_mask:0xf\n"
    " v_add_f32_dpp v72, v72, v72 quad_perm:[1,0,3,2] row_mask:0xf bank_mask:0xf\n"
    " v_add_f32_dpp v73, v73, v73 quad_perm:[1,0,3,2] row_mask:0xf bank_mask:0xf\n"
    " v_add_f32_dpp v64, v64, v64 quad_perm:[2,3,0,1] row_mask:0xf bank_mask:0xf\n"
    " v_add_f32_dpp v65, v65, v65 quad_perm:[2,3,0,1] row_mask:0xf bank_mask:0xf\n"
    " v_add_f32_dpp v72, v72, v72 quad_perm:[2,3,0,1] row_mask:0xf bank_mask:0xf\n"
    " v_add_f32_dpp v73, v73, v73 quad_perm:[2,3,0,1] row_mask:0xf bank_mask:0xf\n"
    " v_add_f32_dpp v64, v64, v64 row_half_mirror row_mask:0xf bank_mask:0xf\n"
    " v_add_f32_dpp v65, v65, v65 row_half_mirror row_mask:0xf bank_mask:0xf\n"
    " v_add_f32_dpp v72, v72, v72 row_half_mirror row_mask:0xf bank_mask:0xf\n"
    " v_add_f32_dpp v73, v73, v73 row_half_mirror row_mask:0xf bank_mask:0xf\n"
    " v_add_f32_dpp v64, v64, v64 row_mirror row_mask:0xf bank_mask:0xf\n"
    " v_add_f32_dpp v65, v65, v65 row_mirror row_mask:0xf bank_mask:0xf\n"
    " v_add_f32_dpp v72, v72, v72 row_mirror row_mask:0xf bank_mask:0xf\n"
    " v_add_f32_dpp v73, v73, v73 row_mirror row_mask:0xf bank_mask:0xf\n"
    " s_mov_b32 s65, 0x3b000000\n"
    " s_mov_b32 s66, 0xffffff00\n"
    " s_and_b32 s67, s50, 0xff\n"
    " v_mov_b32 v21, s67\n"
    " v_mul_f32 v24, s65, v64\n"
    " v_mul_f32 v25, s65, v72\n"
    " v_fma_f32 v25, -v24, v24, v25\n"
    " v_max_f32 v25, 0, v25\n"
    " v_add_f32 v25, 0x3727c5ac, v25\n"
    " v_rsq_f32 v25, v25\n"
    " v_mul_f32 v26, s65, v65\n"
    " v_mul_f32 v27, s65, v73\n"
    " v_fma_f32 v27, -v26, v26, v27\n"
    " v_max_f32 v27, 0, v27\n"
    " v_add_f32 v27, 0x3727c5ac, v27\n"
    " v_rsq_f32 v27, v27\n"
    " v_and_or_b32 v24, v24, s66, v21\n"
    " v_and_or_b32 v25, v25, s66, v21\n"
    " v_and_or_b32 v26, v26, s66, v21\n"
    " v_and_or_b32 v27, v27, s66, v21\n"
    " s_lshl_b32 s60, s47, 6\n"
    " s_add_u32 s60, s60, 757760\n"
    " v_add_u32 v5, s60, v6\n"
    " s_mov_b32 exec_lo, 0x00010001\n"
    " s_mov_b32 exec_hi, 0x00010001\n"
    " buffer_store_dwordx4 v[24:27], v5, s[12:15], 0 offen sc1\n"
    " s_mov_b64 exec, -1\n"
    ".Lhjoin:\n"
    " s_and_b32 s68, s44, 1\n"
    " s_lshl_b32 s60, s45, 12\n"
    " s_lshl_b32 s61, s68, 11\n"
    " s_add_u32 s60, s60, s61\n"
    " s_add_u32 s60, s60, 200704\n"
    " v_lshl_add_u32 v5, v16, 7, s60\n"
    " s_lshl_b32 s62, s68, 13\n"
    " s_add_u32 s62, s62, 757760\n"
    " s_add_u32 s63, s62, 4096\n"
    " s_lshl_b32 s60, s47, 13\n"
    " s_add_u32 s60, s60, s61\n"
    " s_add_u32 s60, s60, 3145728\n"
    " s_add_u32 s64, s60, 4096\n"
    " v_mov_b32 v124, 141824\n"
    ".Llw_17:\n"
    " s_sleep 2\n"
    " ds_read_b32 v125, v124 offset:4\n"
    " s_waitcnt lgkmcnt(0)\n"
    " v_cmp_gt_u32 vcc, 4, v125\n"
    " s_cbranch_vccnz .Llw_17\n"
    " s_sub_u32 s65, 69616, s61\n"
    " v_sub_u32 v124, s65, v1\n"
    " ds_read_b128 v[8:11], v124 offset:1024\n"
    " ds_read_b128 v[12:15], v124 offset:5632\n"
    " ds_read_b128 v[16:19], v124 offset:0\n"
    " ds_read_b128 v[20:23], v124 offset:4608\n"
    " s_lshl_b32 s65, s56, 16\n"
    " s_and_b32 s66, s56, s53\n"
    " s_waitcnt lgkmcnt(3)\n"
    " v_and_b32 v56, s53, v11\n"
    " v_lshlrev_b32 v57, 16, v11\n"
    " v_and_b32 v58, s53, v10\n"
    " v_lshlrev_b32 v59, 16, v10\n"
    " v_and_b32 v60, s53, v9\n"
    " v_lshlrev_b32 v61, 16, v9\n"
    " v_and_b32 v62, s53, v8\n"
    " v_lshlrev_b32 v63, 16, v8\n"
    " v_mul_f32 v56, s65, v56\n"
    " v_mul_f32 v57, s65, v57\n"
    " v_mul_f32 v58, s65, v58\n"
    " v_mul_f32 v59, s65, v59\n"
    " v_mul_f32 v60, s65, v60\n"
    " v_mul_f32 v61, s65, v61\n"
    " v_mul_f32 v62, s65, v62\n"
    " v_mul_f32 v63, s65, v63\n"
    " s_waitcnt lgkmcnt(2)\n"
    " v_and_b32 v64, s53, v15\n"
    " v_lshlrev_b32 v65, 16, v15\n"
    " v_and_b32 v66, s53, v14\n"
    " v_lshlrev_b32 v67, 16, v14\n"
    " v_and_b32 v68, s53, v13\n"
    " v_lshlrev_b32 v69, 16, v13\n"
    " v_and_b32 v70, s53, v12\n"
    " v_lshlrev_b32 v71, 16, v12\n"
    " v_mul_f32 v64, s66, v64\n"
    " v_mul_f32 v65, s66, v65\n"
    " v_mul_f32 v66, s66, v66\n"
    " v_mul_f32 v67, s66, v67\n"
    " v_mul_f32 v68, s66, v68\n"
    " v_mul_f32 v69, s66, v69\n"
    " v_mul_f32 v70, s66, v70\n"
    " v_mul_f32 v71, s66, v71\n"
    " s_waitcnt lgkmcnt(1)\n"
    " v_and_b32 v72, s53, v19\n"
    " v_lshlrev_b32 v73, 16, v19\n"
    " v_and_b32 v74, s53, v18\n"
    " v_lshlrev_b32 v75, 16, v18\n"
    " v_and_b32 v76, s53, v17\n"
    " v_lshlrev_b32 v77, 16, v17\n"
    " v_and_b32 v78, s53, v16\n"
    " v_lshlrev_b32 v79, 16, v16\n"
    " v_mul_f32 v72, s65, v72\n"
    " v_mul_f32 v73, s65, v73\n"
    " v_mul_f32 v74, s65, v74\n"
    " v_mul_f32 v75, s65, v75\n"
    " v_mul_f32 v76, s65, v76\n"
    " v_mul_f32 v77, s65, v77\n"
    " v_mul_f32 v78, s65, v78\n"
    " v_mul_f32 v79, s65, v79\n"
    " s_waitcnt lgkmcnt(0)\n"
    " v_and_b32 v80, s53, v23\n"
    " v_lshlrev_b32 v81, 16, v23\n"
    " v_and_b32 v82, s53, v22\n"
    " v_lshlrev_b32 v83, 16, v22\n"
    " v_and_b32 v84, s53, v21\n"
    " v_lshlrev_b32 v85, 16, v21\n"
    " v_and_b32 v86, s53, v20\n"
    " v_lshlrev_b32 v87, 16, v20\n"
    " v_mul_f32 v80, s66, v80\n"
    " v_mul_f32 v81, s66, v81\n"
    " v_mul_f32 v82, s66, v82\n"
    " v_mul_f32 v83, s66, v83\n"
    " v_mul_f32 v84, s66, v84\n"
    " v_mul_f32 v85, s66, v85\n"
    " v_mul_f32 v86, s66, v86\n"
    " v_mul_f32 v87, s66, v87\n"
    " s_lshl_b32 s65, s68, 12\n"
    " s_add_u32 s65, s65, 75776\n"
    " v_and_b32 v125, 1, v0\n"
    " v_mul_u32_u24 v125, 40, v125\n"
    " v_bfe_u32 v124, v0, 1, 4\n"
    " v_xor_b32 v125, v125, v124\n"
    " v_bfe_u32 v124, v0, 5, 1\n"
    " v_lshl_add_u32 v125, v124, 6, v125\n"
    " v_lshl_add_u32 v120, v125, 4, s65\n"
    " s_lshr_b32 s65, s47, 1\n"
    " s_and_b32 s65, s65, 2\n"
    " s_and_b32 s66, s47, 1\n"
    " s_or_b32 s65, s65, s66\n"
    " s_lshl_b32 s65, s65, 5\n"
    " s_lshl_b32 s66, s68, 20\n"
    " s_lshl_b32 s67, s47, 8\n"
    " s_add_u32 s66, s66, s67\n"
    " s_add_u32 s66, s66, 5242880\n"
    " v_and_b32 v125, 0x70, v1\n"
    " v_xor_b32 v125, s65, v125\n"
    " v_lshl_add_u32 v125, v7, 16, v125\n"
    " v_add_u32 v121, s66, v125\n"
    " v_add_u32 v122, 0x80000, v121\n"
    ".Lfp_18:\n"
    " buffer_load_dwordx2 v[126:127], v5, s[12:15], 0 offen sc1\n"
    " s_waitcnt vmcnt(0)\n"
    " v_xor_b32 v126, s50, v126\n"
    " v_xor_b32 v127, s51, v127\n"
    " v_or_b32 v126, v126, v127\n"
    " v_cmp_ne_u32 vcc, 0, v126\n"
    " s_cbranch_vccz .Lsp_19\n"
    " s_sleep 4\n"
    " s_sub_u32 s57, s57, 1\n"
    " s_cbranch_scc0 .Lfp_18\n"
    " s_mov_b32 s57, 0\n"
    ".Lsp_19:\n"
    " buffer_load_dwordx4 v[8:11], v1, s[12:15], s60 offen offset:0 sc1\n"
    " buffer_load_dwordx4 v[12:15], v1, s[12:15], s64 offen offset:0 sc1\n"
    " buffer_load_dwordx4 v[16:19], v1, s[12:15], s60 offen offset:1024 sc1\n"
    " buffer_load_dwordx4 v[20:23], v1, s[12:15], s64 offen offset:1024 sc1\n"
    ".Lst_20:\n"
    " buffer_load_dwordx4 v[128:131], v1, s[12:15], s62 offen offset:0 sc1\n"
    " buffer_load_dwordx4 v[132:135], v1, s[12:15], s62 offen offset:1024 sc1\n"
    " buffer_load_dwordx4 v[136:139], v1, s[12:15], s62 offen offset:2048 sc1\n"
    " buffer_load_dwordx4 v[140:143], v1, s[12:15], s62 offen offset:3072 sc1\n"
    " buffer_load_dwordx4 v[144:147], v1, s[12:15], s63 offen offset:0 sc1\n"
    " buffer_load_dwordx4 v[148:151], v1, s[12:15], s63 offen offset:1024 sc1\n"
    " buffer_load_dwordx4 v[152:155], v1, s[12:15], s63 offen offset:2048 sc1\n"
    " buffer_load_dwordx4 v[156:159], v1, s[12:15], s63 offen offset:3072 sc1\n"
    " v_and_b32 v125, 3, v0\n"
    " v_mul_u32_u24 v125, 2080, v125\n"
    " v_bfe_u32 v124, v0, 2, 4\n"
    " v_lshl_add_u32 v125, v124, 4, v125\n"
    " v_add_u32 v125, s69, v125\n"
    " s_waitcnt vmcnt(11)\n"
    " v_lshlrev_b32 v192, 16, v8\n"
    " v_and_b32 v193, s53, v8\n"
    " v_lshlrev_b32 v194, 16, v9\n"
    " v_and_b32 v195, s53, v9\n"
    " v_lshlrev_b32 v196, 16, v10\n"
    " v_and_b32 v197, s53, v10\n"
    " v_lshlrev_b32 v198, 16, v11\n"
    " v_and_b32 v199, s53, v11\n"
    " s_waitcnt vmcnt(10)\n"
    " v_lshlrev_b32 v200, 16, v12\n"
    " v_and_b32 v201, s53, v12\n"
    " v_lshlrev_b32 v202, 16, v13\n"
    " v_and_b32 v203, s53, v13\n"
    " v_lshlrev_b32 v204, 16, v14\n"
    " v_and_b32 v205, s53, v14\n"
    " v_lshlrev_b32 v206, 16, v15\n"
    " v_and_b32 v207, s53, v15\n"
    " s_waitcnt vmcnt(9)\n"
    " v_lshlrev_b32 v208, 16, v16\n"
    " v_and_b32 v209, s53, v16\n"
    " v_lshlrev_b32 v210, 16, v17\n"
    " v_and_b32 v211, s53, v17\n"
    " v_lshlrev_b32 v212, 16, v18\n"
    " v_and_b32 v213, s53, v18\n"
    " v_lshlrev_b32 v214, 16, v19\n"
    " v_and_b32 v215, s53, v19\n"
    " s_waitcnt vmcnt(8)\n"
    " v_lshlrev_b32 v216, 16, v20\n"
    " v_and_b32 v217, s53, v20\n"
    " v_lshlrev_b32 v218, 16, v21\n"
    " v_and_b32 v219, s53, v21\n"
    " v_lshlrev_b32 v220, 16, v22\n"
    " v_and_b32 v221, s53, v22\n"
    " v_lshlrev_b32 v222, 16, v23\n"
    " v_and_b32 v223, s53, v23\n"
    " s_waitcnt vmcnt(7)\n"
    " ds_write_b128 v125, v[128:131] offset:0\n"
    " v_xor_b32 v5, s50, v128\n"
    " v_bitop3_b32 v5, v129, s50, v5 bitop3:0xbe\n"
    " v_bitop3_b32 v5, v130, s50, v5 bitop3:0xbe\n"
    " v_bitop3_b32 v5, v131, s50, v5 bitop3:0xbe\n"
    " s_waitcnt vmcnt(6)\n"
    " ds_write_b128 v125, v[132:135] offset:256\n"
    " v_bitop3_b32 v5, v132, s50, v5 bitop3:0xbe\n"
    " v_bitop3_b32 v5, v133, s50, v5 bitop3:0xbe\n"
    " v_bitop3_b32 v5, v134, s50, v5 bitop3:0xbe\n"
    " v_bitop3_b32 v5, v135, s50, v5 bitop3:0xbe\n"
    " s_waitcnt vmcnt(5)\n"
    " ds_write_b128 v125, v[136:139] offset:512\n"
    " v_bitop3_b32 v5, v136, s50, v5 bitop3:0xbe\n"
    " v_bitop3_b32 v5, v137, s50, v5 bitop3:0xbe\n"
    " v_bitop3_b32 v5, v138, s50, v5 bitop3:0xbe\n"
    " v_bitop3_b32 v5, v139, s50, v5 bitop3:0xbe\n"
    " s_waitcnt vmcnt(4)\n"
    " ds_write_b128 v125, v[140:143] offset:768\n"
    " v_bitop3_b32 v5, v140, s50, v5 bitop3:0xbe\n"
    " v_bitop3_b32 v5, v141, s50, v5 bitop3:0xbe\n"
    " v_bitop3_b32 v5, v142, s50, v5 bitop3:0xbe\n"
    " v_bitop3_b32 v5, v143, s50, v5 bitop3:0xbe\n"
    " s_waitcnt vmcnt(3)\n"
    " ds_write_b128 v125, v[144:147] offset:1024\n"
    " v_bitop3_b32 v5, v144, s50, v5 bitop3:0xbe\n"
    " v_bitop3_b32 v5, v145, s50, v5 bitop3:0xbe\n"
    " v_bitop3_b32 v5, v146, s50, v5 bitop3:0xbe\n"
    " v_bitop3_b32 v5, v147, s50, v5 bitop3:0xbe\n"
    " s_waitcnt vmcnt(2)\n"
    " ds_write_b128 v125, v[148:151] offset:1280\n"
    " v_bitop3_b32 v5, v148, s50, v5 bitop3:0xbe\n"
    " v_bitop3_b32 v5, v149, s50, v5 bitop3:0xbe\n"
    " v_bitop3_b32 v5, v150, s50, v5 bitop3:0xbe\n"
    " v_bitop3_b32 v5, v151, s50, v5 bitop3:0xbe\n"
    " s_waitcnt vmcnt(1)\n"
    " ds_write_b128 v125, v[152:155] offset:1536\n"
    " v_bitop3_b32 v5, v152, s50, v5 bitop3:0xbe\n"
    " v_bitop3_b32 v5, v153, s50, v5 bitop3:0xbe\n"
    " v_bitop3_b32 v5, v154, s50, v5 bitop3:0xbe\n"
    " v_bitop3_b32 v5, v155, s50, v5 bitop3:0xbe\n"
    " s_waitcnt vmcnt(0)\n"
    " ds_write_b128 v125, v[156:159] offset:1792\n"
    " v_bitop3_b32 v5, v156, s50, v5 bitop3:0xbe\n"
    " v_bitop3_b32 v5, v157, s50, v5 bitop3:0xbe\n"
    " v_bitop3_b32 v5, v158, s50, v5 bitop3:0xbe\n"
    " v_bitop3_b32 v5, v159, s50, v5 bitop3:0xbe\n"
    " v_and_b32 v5, 0xff, v5\n"
    " v_cmp_ne_u32 vcc, 0, v5\n"
    " s_cbranch_vccz .Lsp_21\n"
    " s_sleep 4\n"
    " s_sub_u32 s57, s57, 1\n"
    " s_cbranch_scc0 .Lst_20\n"
    " s_mov_b32 s57, 0\n"
    ".Lsp_21:\n"
    " v_add_u32 v5, s69, v1\n"
    " s_mov_b32 s65, 0xffffff00\n"
    " ds_read_b128 v[160:163], v5 offset:0\n"
    " ds_read_b128 v[164:167], v5 offset:2080\n"
    " ds_read_b128 v[168:171], v5 offset:4160\n"
    " ds_read_b128 v[172:175], v5 offset:6240\n"
    " ds_read_b128 v[176:179], v5 offset:1024\n"
    " ds_read_b128 v[180:183], v5 offset:3104\n"
    " ds_read_b128 v[184:187], v5 offset:5184\n"
    " ds_read_b128 v[188:191], v5 offset:7264\n"
    " s_waitcnt lgkmcnt(7)\n"
    " v_and_b32 v160, s65, v160\n"
    " v_and_b32 v161, s65, v161\n"
    " v_and_b32 v162, s65, v162\n"
    " v_and_b32 v163, s65, v163\n"
    " s_waitcnt lgkmcnt(6)\n"
    " v_and_b32 v164, s65, v164\n"
    " v_and_b32 v165, s65, v165\n"
    " v_and_b32 v166, s65, v166\n"
    " v_and_b32 v167, s65, v167\n"
    " s_waitcnt lgkmcnt(5)\n"
    " v_and_b32 v168, s65, v168\n"
    " v_and_b32 v169, s65, v169\n"
    " v_and_b32 v170, s65, v170\n"
    " v_and_b32 v171, s65, v171\n"
    " s_waitcnt lgkmcnt(4)\n"
    " v_and_b32 v172, s65, v172\n"
    " v_and_b32 v173, s65, v173\n"
    " v_and_b32 v174, s65, v174\n"
    " v_and_b32 v175, s65, v175\n"
    " v_sub_f32 v24, v192, v160\n"
    " v_sub_f32 v25, v193, v162\n"
    " v_sub_f32 v26, v194, v164\n"
    " v_sub_f32 v27, v195, v166\n"
    " v_sub_f32 v28, v196, v168\n"
    " v_sub_f32 v29, v197, v170\n"
    " v_sub_f32 v30, v198, v172\n"
    " v_sub_f32 v31, v199, v174\n"
    " v_mul_f32 v24, v24, v161\n"
    " v_mul_f32 v25, v25, v163\n"
    " v_mul_f32 v26, v26, v165\n"
    " v_mul_f32 v27, v27, v167\n"
    " v_mul_f32 v28, v28, v169\n"
    " v_mul_f32 v29, v29, v171\n"
    " v_mul_f32 v30, v30, v173\n"
    " v_mul_f32 v31, v31, v175\n"
    " v_sub_f32 v32, v200, v160\n"
    " v_sub_f32 v33, v201, v162\n"
    " v_sub_f32 v34, v202, v164\n"
    " v_sub_f32 v35, v203, v166\n"
    " v_sub_f32 v36, v204, v168\n"
    " v_sub_f32 v37, v205, v170\n"
    " v_sub_f32 v38, v206, v172\n"
    " v_sub_f32 v39, v207, v174\n"
    " v_mul_f32 v32, v32, v161\n"
    " v_mul_f32 v33, v33, v163\n"
    " v_mul_f32 v34, v34, v165\n"
    " v_mul_f32 v35, v35, v167\n"
    " v_mul_f32 v36, v36, v169\n"
    " v_mul_f32 v37, v37, v171\n"
    " v_mul_f32 v38, v38, v173\n"
    " v_mul_f32 v39, v39, v175\n"
    " s_waitcnt lgkmcnt(3)\n"
    " v_and_b32 v176, s65, v176\n"
    " v_and_b32 v177, s65, v177\n"
    " v_and_b32 v178, s65, v178\n"
    " v_and_b32 v179, s65, v179\n"
    " s_waitcnt lgkmcnt(2)\n"
    " v_and_b32 v180, s65, v180\n"
    " v_and_b32 v181, s65, v181\n"
    " v_and_b32 v182, s65, v182\n"
    " v_and_b32 v183, s65, v183\n"
    " s_waitcnt lgkmcnt(1)\n"
    " v_and_b32 v184, s65, v184\n"
    " v_and_b32 v185, s65, v185\n"
    " v_and_b32 v186, s65, v186\n"
    " v_and_b32 v187, s65, v187\n"
    " s_waitcnt lgkmcnt(0)\n"
    " v_and_b32 v188, s65, v188\n"
    " v_and_b32 v189, s65, v189\n"
    " v_and_b32 v190, s65, v190\n"
    " v_and_b32 v191, s65, v191\n"
    " v_sub_f32 v40, v208, v176\n"
    " v_sub_f32 v41, v209, v178\n"
    " v_sub_f32 v42, v210, v180\n"
    " v_sub_f32 v43, v211, v182\n"
    " v_sub_f32 v44, v212, v184\n"
    " v_sub_f32 v45, v213, v186\n"
    " v_sub_f32 v46, v214, v188\n"
    " v_sub_f32 v47, v215, v190\n"
    " v_mul_f32 v40, v40, v177\n"
    " v_mul_f32 v41, v41, v179\n"
    " v_mul_f32 v42, v42, v181\n"
    " v_mul_f32 v43, v43, v183\n"
    " v_mul_f32 v44, v44, v185\n"
    " v_mul_f32 v45, v45, v187\n"
    " v_mul_f32 v46, v46, v189\n"
    " v_mul_f32 v47, v47, v191\n"
    " v_sub_f32 v48, v216, v176\n"
    " v_sub_f32 v49, v217, v178\n"
    " v_sub_f32 v50, v218, v180\n"
    " v_sub_f32 v51, v219, v182\n"
    " v_sub_f32 v52, v220, v184\n"
    " v_sub_f32 v53, v221, v186\n"
    " v_sub_f32 v54, v222, v188\n"
    " v_sub_f32 v55, v223, v190\n"
    " v_mul_f32 v48, v48, v177\n"
    " v_mul_f32 v49, v49, v179\n"
    " v_mul_f32 v50, v50, v181\n"
    " v_mul_f32 v51, v51, v183\n"
    " v_mul_f32 v52, v52, v185\n"
    " v_mul_f32 v53, v53, v187\n"
    " v_mul_f32 v54, v54, v189\n"
    " v_mul_f32 v55, v55, v191\n"
    " v_pk_fma_f32 v[56:57], v[24:25], v[56:57], v[192:193]\n"
    " v_pk_fma_f32 v[58:59], v[26:27], v[58:59], v[194:195]\n"
    " v_pk_fma_f32 v[60:61], v[28:29], v[60:61], v[196:197]\n"
    " v_pk_fma_f32 v[62:63], v[30:31], v[62:63], v[198:199]\n"
    " v_pk_fma_f32 v[64:65], v[32:33], v[64:65], v[200:201]\n"
    " v_pk_fma_f32 v[66:67], v[34:35], v[66:67], v[202:203]\n"
    " v_pk_fma_f32 v[68:69], v[36:37], v[68:69], v[204:205]\n"
    " v_pk_fma_f32 v[70:71], v[38:39], v[70:71], v[206:207]\n"
    " v_pk_fma_f32 v[72:73], v[40:41], v[72:73], v[208:209]\n"
    " v_pk_fma_f32 v[74:75], v[42:43], v[74:75], v[210:211]\n"
    " v_pk_fma_f32 v[76:77], v[44:45], v[76:77], v[212:213]\n"
    " v_pk_fma_f32 v[78:79], v[46:47], v[78:79], v[214:215]\n"
    " v_pk_fma_f32 v[80:81], v[48:49], v[80:81], v[216:217]\n"
    " v_pk_fma_f32 v[82:83], v[50:51], v[82:83], v[218:219]\n"
    " v_pk_fma_f32 v[84:85], v[52:53], v[84:85], v[220:221]\n"
    " v_pk_fma_f32 v[86:87], v[54:55], v[86:87], v[222:223]\n"
    " v_mov_b32 v124, 141824\n"
    ".Llw_22:\n"
    " ds_read_b32 v125, v124 offset:12\n"
    " s_waitcnt lgkmcnt(0)\n"
    " v_cmp_gt_u32 vcc, 4, v125\n"
    " s_cbranch_vccnz .Llw_22\n"
    " ds_read_b128 v[128:131], v120 offset:0\n"
    " ds_read_b128 v[132:135], v120 offset:256\n"
    " ds_read_b128 v[136:139], v120 offset:8192\n"
    " ds_read_b128 v[140:143], v120 offset:8448\n"
    " ds_read_b128 v[144:147], v120 offset:16384\n"
    " ds_read_b128 v[148:151], v120 offset:16640\n"
    " ds_read_b128 v[152:155], v120 offset:24576\n"
    " ds_read_b128 v[156:159], v120 offset:24832\n"
    " ds_read_b128 v[160:163], v120 offset:2048\n"
    " ds_read_b128 v[164:167], v120 offset:2304\n"
    " ds_read_b128 v[168:171], v120 offset:10240\n"
    " ds_read_b128 v[172:175], v120 offset:10496\n"
    " ds_read_b128 v[176:179], v120 offset:18432\n"
    " ds_read_b128 v[180:183], v120 offset:18688\n"
    " ds_read_b128 v[184:187], v120 offset:26624\n"
    " ds_read_b128 v[188:191], v120 offset:26880\n"
    " s_waitcnt lgkmcnt(13)\n"
    " v_pk_add_f32 v[128:129], v[128:129], v[136:137]\n"
    " v_pk_add_f32 v[130:131], v[130:131], v[138:139]\n"
    " s_waitcnt lgkmcnt(12)\n"
    " v_pk_add_f32 v[132:133], v[132:133], v[140:141]\n"
    " v_pk_add_f32 v[134:135], v[134:135], v[142:143]\n"
    " v_pk_fma_f32 v[56:57], v[24:25], v[128:129], v[56:57]\n"
    " v_pk_fma_f32 v[58:59], v[26:27], v[130:131], v[58:59]\n"
    " v_pk_fma_f32 v[60:61], v[28:29], v[132:133], v[60:61]\n"
    " v_pk_fma_f32 v[62:63], v[30:31], v[134:135], v[62:63]\n"
    " v_cvt_pk_bf16_f32 v8, v56, v57\n"
    " v_cvt_pk_bf16_f32 v9, v58, v59\n"
    " v_cvt_pk_bf16_f32 v10, v60, v61\n"
    " v_cvt_pk_bf16_f32 v11, v62, v63\n"
    " v_min_u32 v8, -2, v8\n"
    " v_min_u32 v9, -2, v9\n"
    " v_min_u32 v10, -2, v10\n"
    " v_min_u32 v11, -2, v11\n"
    " buffer_store_dwordx4 v[8:11], v121, s[12:15], 0 offen offset:0 sc1\n"
    " s_waitcnt lgkmcnt(9)\n"
    " v_pk_add_f32 v[144:145], v[144:145], v[152:153]\n"
    " v_pk_add_f32 v[146:147], v[146:147], v[154:155]\n"
    " s_waitcnt lgkmcnt(8)\n"
    " v_pk_add_f32 v[148:149], v[148:149], v[156:157]\n"
    " v_pk_add_f32 v[150:151], v[150:151], v[158:159]\n"
    " v_pk_fma_f32 v[64:65], v[32:33], v[144:145], v[64:65]\n"
    " v_pk_fma_f32 v[66:67], v[34:35], v[146:147], v[66:67]\n"
    " v_pk_fma_f32 v[68:69], v[36:37], v[148:149], v[68:69]\n"
    " v_pk_fma_f32 v[70:71], v[38:39], v[150:151], v[70:71]\n"
    " v_cvt_pk_bf16_f32 v12, v64, v65\n"
    " v_cvt_pk_bf16_f32 v13, v66, v67\n"
    " v_cvt_pk_bf16_f32 v14, v68, v69\n"
    " v_cvt_pk_bf16_f32 v15, v70, v71\n"
    " v_min_u32 v12, -2, v12\n"
    " v_min_u32 v13, -2, v13\n"
    " v_min_u32 v14, -2, v14\n"
    " v_min_u32 v15, -2, v15\n"
    " buffer_store_dwordx4 v[12:15], v121, s[12:15], 0 offen offset:128 sc1\n"
    " s_waitcnt lgkmcnt(5)\n"
    " v_pk_add_f32 v[160:161], v[160:161], v[168:169]\n"
    " v_pk_add_f32 v[162:163], v[162:163], v[170:171]\n"
    " s_waitcnt lgkmcnt(4)\n"
    " v_pk_add_f32 v[164:165], v[164:165], v[172:173]\n"
    " v_pk_add_f32 v[166:167], v[166:167], v[174:175]\n"
    " v_pk_fma_f32 v[72:73], v[40:41], v[160:161], v[72:73]\n"
    " v_pk_fma_f32 v[74:75], v[42:43], v[162:163], v[74:75]\n"
    " v_pk_fma_f32 v[76:77], v[44:45], v[164:165], v[76:77]\n"
    " v_pk_fma_f32 v[78:79], v[46:47], v[166:167], v[78:79]\n"
    " v_cvt_pk_bf16_f32 v16, v72, v73\n"
    " v_cvt_pk_bf16_f32 v17, v74, v75\n"
    " v_cvt_pk_bf16_f32 v18, v76, v77\n"
    " v_cvt_pk_bf16_f32 v19, v78, v79\n"
    " v_min_u32 v16, -2, v16\n"
    " v_min_u32 v17, -2, v17\n"
    " v_min_u32 v18, -2, v18\n"
    " v_min_u32 v19, -2, v19\n"
    " buffer_store_dwordx4 v[16:19], v122, s[12:15], 0 offen offset:0 sc1\n"
    " s_waitcnt lgkmcnt(1)\n"
    " v_pk_add_f32 v[176:177], v[176:177], v[184:185]\n"
    " v_pk_add_f32 v[178:179], v[178:179], v[186:187]\n"
    " s_waitcnt lgkmcnt(0)\n"
    " v_pk_add_f32 v[180:181], v[180:181], v[188:189]\n"
    " v_pk_add_f32 v[182:183], v[182:183], v[190:191]\n"
    " v_pk_fma_f32 v[80:81], v[48:49], v[176:177], v[80:81]\n"
    " v_pk_fma_f32 v[82:83], v[50:51], v[178:179], v[82:83]\n"
    " v_pk_fma_f32 v[84:85], v[52:53], v[180:181], v[84:85]\n"
    " v_pk_fma_f32 v[86:87], v[54:55], v[182:183], v[86:87]\n"
    " v_cvt_pk_bf16_f32 v20, v80, v81\n"
    " v_cvt_pk_bf16_f32 v21, v82, v83\n"
    " v_cvt_pk_bf16_f32 v22, v84, v85\n"
    " v_cvt_pk_bf16_f32 v23, v86, v87\n"
    " v_min_u32 v20, -2, v20\n"
    " v_min_u32 v21, -2, v21\n"
    " v_min_u32 v22, -2, v22\n"
    " v_min_u32 v23, -2, v23\n"
    " buffer_store_dwordx4 v[20:23], v122, s[12:15], 0 offen offset:128 sc1\n"
    " s_cmp_eq_u32 s44, 7\n"
    " s_cbranch_scc1 .Lh7\n"
    " s_setprio 0\n"
    " v_mov_b32 v124, 141824\n"
    ".Llw_23:\n"
    " s_sleep 1\n"
    " ds_read_b32 v125, v124 offset:16\n"
    " s_waitcnt lgkmcnt(0)\n"
    " v_cmp_gt_u32 vcc, 1, v125\n"
    " s_cbranch_vccnz .Llw_23\n"
    " buffer_load_dwordx4 v[128:131], v1, s[12:15], s87 offen offset:0 sc1\n"
    " buffer_load_dwordx4 v[132:135], v1, s[12:15], s87 offen offset:1024 sc1\n"
    " buffer_load_dwordx4 v[136:139], v1, s[12:15], s87 offen offset:2048 sc1\n"
    " buffer_load_dwordx4 v[140:143], v1, s[12:15], s87 offen offset:3072 sc1\n"
    " s_add_u32 s87, s87, 4096\n"
    " buffer_load_dwordx4 v[144:147], v1, s[12:15], s87 offen offset:0 sc1\n"
    " buffer_load_dwordx4 v[148:151], v1, s[12:15], s87 offen offset:1024 sc1\n"
    " buffer_load_dwordx4 v[152:155], v1, s[12:15], s87 offen offset:2048 sc1\n"
    " buffer_load_dwordx4 v[156:159], v1, s[12:15], s87 offen offset:3072 sc1\n"
    " s_add_u32 s87, s87, 4096\n"
    " buffer_load_dwordx4 v[160:163], v1, s[12:15], s87 offen offset:0 sc1\n"
    " buffer_load_dwordx4 v[164:167], v1, s[12:15], s87 offen offset:1024 sc1\n"
    " buffer_load_dwordx4 v[168:171], v1, s[12:15], s87 offen offset:2048 sc1\n"
    " buffer_load_dwordx4 v[172:175], v1, s[12:15], s87 offen offset:3072 sc1\n"
    " s_add_u32 s87, s87, 4096\n"
    " buffer_load_dwordx4 v[176:179], v1, s[12:15], s87 offen offset:0 sc1\n"
    " buffer_load_dwordx4 v[180:183], v1, s[12:15], s87 offen offset:1024 sc1\n"
    " buffer_load_dwordx4 v[184:187], v1, s[12:15], s87 offen offset:2048 sc1\n"
    " buffer_load_dwordx4 v[188:191], v1, s[12:15], s87 offen offset:3072 sc1\n"
    " s_add_u32 s87, s87, 4096\n"
    " buffer_load_dwordx4 v[192:195], v1, s[12:15], s87 offen offset:0 sc1\n"
    " buffer_load_dwordx4 v[196:199], v1, s[12:15], s87 offen offset:1024 sc1\n"
    " buffer_load_dwordx4 v[200:203], v1, s[12:15], s87 offen offset:2048 sc1\n"
    " buffer_load_dwordx4 v[204:207], v1, s[12:15], s87 offen offset:3072 sc1\n"
    " s_add_u32 s87, s87, 4096\n"
    " buffer_load_dwordx4 v[208:211], v1, s[12:15], s87 offen offset:0 sc1\n"
    " buffer_load_dwordx4 v[212:215], v1, s[12:15], s87 offen offset:1024 sc1\n"
    " buffer_load_dwordx4 v[216:219], v1, s[12:15], s87 offen offset:2048 sc1\n"
    " buffer_load_dwordx4 v[220:223], v1, s[12:15], s87 offen offset:3072 sc1\n"
    " s_waitcnt vmcnt(24)\n"
    ".Lpre:\n"
    " s_lshr_b32 s61, s86, 2\n"
    " v_and_b32 v6, 15, v0\n"
    " s_lshl_b32 s61, s61, 13\n"
    " s_and_b32 s62, s86, 3\n"
    " s_lshl_b32 s62, s62, 7\n"
    " s_add_u32 s61, s61, s62\n"
    " s_add_u32 s61, s61, 774144\n"
    " v_bfe_u32 v2, v0, 4, 2\n"
    " v_lshlrev_b32 v3, 11, v2\n"
    " v_lshl_add_u32 v3, v6, 2, v3\n"
    " s_lshl_b32 s69, s48, 16\n"
    " s_lshl_b32 s65, s44, 13\n"
    " s_add_u32 s69, s69, s65\n"
    " s_add_u32 s69, s69, 5242880\n"
    " s_add_u32 s66, s69, 4096\n"
    " v_bfe_u32 v6, v0, 2, 2\n"
    " v_lshl_add_u32 v2, v2, 3, v6\n"
    " v_and_b32 v6, 3, v0\n"
    " v_lshlrev_b32 v6, 3, v6\n"
    " v_lshl_add_u32 v2, v2, 7, v6\n"
    " v_bfe_u32 v6, v0, 3, 2\n"
    " v_lshl_add_u32 v48, v6, 5, v2\n"
    " v_xor_b32 v49, 32, v48\n"
    " v_xor_b32 v50, 64, v48\n"
    " v_xor_b32 v51, 96, v48\n"
    " v_add_u32 v7, s65, v1\n"
    " s_add_u32 s67, s49, s44\n"
    " s_add_u32 s67, s67, 1\n"
    " s_and_b32 s67, s67, 7\n"
    " s_mul_i32 s58, s67, 7\n"
    " s_mul_i32 s68, s49, 56\n"
    " s_add_u32 s68, s68, s58\n"
    " s_lshl_b32 s68, s68, 12\n"
    " s_add_u32 s68, s68, 9175040\n"
    " s_mul_i32 s62, s48, 224\n"
    " s_add_u32 s70, s62, s86\n"
    " s_lshl_b32 s70, s70, 10\n"
    " s_add_u32 s70, s70, 11010048\n"
    " s_add_u32 s71, s62, s58\n"
    " s_lshl_b32 s71, s71, 10\n"
    " s_add_u32 s71, s71, 11010048\n"
    " s_add_u32 s72, s71, 0\n"
    " s_add_u32 s73, s71, 4096\n"
    " s_add_u32 s74, s71, 57344\n"
    " s_add_u32 s75, s71, 61440\n"
    " s_add_u32 s76, s71, 114688\n"
    " s_add_u32 s77, s71, 118784\n"
    " s_add_u32 s78, s71, 172032\n"
    " s_add_u32 s79, s71, 176128\n"
    " s_barrier\n"
    " buffer_load_dwordx4 v[8:11], v1, s[12:15], s69 offen offset:0 sc1\n"
    " buffer_load_dwordx4 v[12:15], v1, s[12:15], s69 offen offset:1024 sc1\n"
    " buffer_load_dwordx4 v[16:19], v1, s[12:15], s69 offen offset:2048 sc1\n"
    " buffer_load_dwordx4 v[20:23], v1, s[12:15], s69 offen offset:3072 sc1\n"
    " buffer_load_dwordx4 v[24:27], v1, s[12:15], s66 offen offset:0 sc1\n"
    " buffer_load_dwordx4 v[28:31], v1, s[12:15], s66 offen offset:1024 sc1\n"
    " buffer_load_dwordx4 v[32:35], v1, s[12:15], s66 offen offset:2048 sc1\n"
    " buffer_load_dwordx4 v[36:39], v1, s[12:15], s66 offen offset:3072 sc1\n"
    " s_barrier\n"
    " s_cmp_eq_u32 s44, 7\n"
    " s_cbranch_scc1 .Lmid7\n"
    " s_add_u32 s87, s87, 4096\n"
    " buffer_load_dwordx4 v[224:227], v1, s[12:15], s87 offen offset:0 sc1\n"
    " s_waitcnt vmcnt(8)\n"
    " ds_write_b128 v7, v[8:11] offset:0\n"
    " v_max3_u32 v5, v8, v9, v10\n"
    " v_max3_u32 v5, v10, v11, v5\n"
    " buffer_load_dwordx4 v[228:231], v1, s[12:15], s87 offen offset:1024 sc1\n"
    " s_waitcnt vmcnt(8)\n"
    " ds_write_b128 v7, v[12:15] offset:1024\n"
    " v_max3_u32 v5, v12, v13, v5\n"
    " v_max3_u32 v5, v14, v15, v5\n"
    " buffer_load_dwordx4 v[232:235], v1, s[12:15], s87 offen offset:2048 sc1\n"
    " s_waitcnt vmcnt(8)\n"
    " ds_write_b128 v7, v[16:19] offset:2048\n"
    " v_max3_u32 v5, v16, v17, v5\n"
    " v_max3_u32 v5, v18, v19, v5\n"
    " buffer_load_dwordx4 v[236:239], v1, s[12:15], s87 offen offset:3072 sc1\n"
    " s_waitcnt vmcnt(8)\n"
    " ds_write_b128 v7, v[20:23] offset:3072\n"
    " v_max3_u32 v5, v20, v21, v5\n"
    " v_max3_u32 v5, v22, v23, v5\n"
    " s_add_u32 s87, s87, 4096\n"
    " buffer_load_dwordx4 v[240:243], v1, s[12:15], s87 offen offset:0 sc1\n"
    " s_waitcnt vmcnt(8)\n"
    " ds_write_b128 v7, v[24:27] offset:4096\n"
    " v_max3_u32 v5, v24, v25, v5\n"
    " v_max3_u32 v5, v26, v27, v5\n"
    " buffer_load_dwordx4 v[244:247], v1, s[12:15], s87 offen offset:1024 sc1\n"
    " s_waitcnt vmcnt(8)\n"
    " ds_write_b128 v7, v[28:31] offset:5120\n"
    " v_max3_u32 v5, v28, v29, v5\n"
    " v_max3_u32 v5, v30, v31, v5\n"
    " buffer_load_dwordx4 v[248:251], v1, s[12:15], s87 offen offset:2048 sc1\n"
    " s_waitcnt vmcnt(8)\n"
    " ds_write_b128 v7, v[32:35] offset:6144\n"
    " v_max3_u32 v5, v32, v33, v5\n"
    " v_max3_u32 v5, v34, v35, v5\n"
    " buffer_load_dwordx4 v[252:255], v1, s[12:15], s87 offen offset:3072 sc1\n"
    " s_waitcnt vmcnt(8)\n"
    " ds_write_b128 v7, v[36:39] offset:7168\n"
    " v_max3_u32 v5, v36, v37, v5\n"
    " v_max3_u32 v5, v38, v39, v5\n"
    " v_cmp_eq_u32 vcc, -1, v5\n"
    " s_cbranch_vccnz .Lhs_115\n"
    ".Lhsb_116:\n"
    " v_and_b32 v7, 15, v0\n"
    " v_lshlrev_b32 v7, 3, v7\n"
    " v_add_u32 v7, 65536, v7\n"
    " s_waitcnt lgkmcnt(0)\n"
    " s_barrier\n"
    " ds_read_b64_tr_b16 v[52:53], v48 offset:0\n"
    " ds_read_b64_tr_b16 v[54:55], v48 offset:512\n"
    " ds_read_b64_tr_b16 v[56:57], v49 offset:0\n"
    " ds_read_b64_tr_b16 v[58:59], v49 offset:512\n"
    " ds_read_b64_tr_b16 v[60:61], v48 offset:4096\n"
    " ds_read_b64_tr_b16 v[62:63], v48 offset:4608\n"
    " ds_read_b64_tr_b16 v[64:65], v49 offset:4096\n"
    " ds_read_b64_tr_b16 v[66:67], v49 offset:4608\n"
    " buffer_load_dword v124, v3, s[12:15], s61 offen offset:0 sc1\n"
    " buffer_load_dword v125, v3, s[12:15], s61 offen offset:512 sc1\n"
    " buffer_load_dword v126, v3, s[12:15], s61 offen offset:1024 sc1\n"
    " buffer_load_dword v127, v3, s[12:15], s61 offen offset:1536 sc1\n"
    " buffer_load_dword v120, v3, s[12:15], s61 offen offset:64 sc1\n"
    " buffer_load_dword v121, v3, s[12:15], s61 offen offset:576 sc1\n"
    " buffer_load_dword v122, v3, s[12:15], s61 offen offset:1088 sc1\n"
    " buffer_load_dword v123, v3, s[12:15], s61 offen offset:1600 sc1\n"
    " ds_read_b64_tr_b16 v[68:69], v48 offset:8192\n"
    " ds_read_b64_tr_b16 v[70:71], v48 offset:8704\n"
    " s_waitcnt lgkmcnt(8)\n"
    " v_mfma_f32_16x16x32_bf16 v[8:11], v[128:131], v[52:55], 0\n"
    " v_mfma_f32_16x16x32_bf16 v[16:19], v[132:135], v[52:55], 0\n"
    " ds_read_b64_tr_b16 v[72:73], v49 offset:8192\n"
    " ds_read_b64_tr_b16 v[74:75], v49 offset:8704\n"
    " s_waitcnt lgkmcnt(8)\n"
    " v_mfma_f32_16x16x32_bf16 v[12:15], v[128:131], v[56:59], 0\n"
    " v_mfma_f32_16x16x32_bf16 v[20:23], v[132:135], v[56:59], 0\n"
    " buffer_load_dwordx4 v[76:79], v1, s[12:15], s68 offen offset:0 sc1\n"
    " ds_read_b64_tr_b16 v[52:53], v48 offset:12288\n"
    " ds_read_b64_tr_b16 v[54:55], v48 offset:12800\n"
    " s_waitcnt lgkmcnt(8)\n"
    " v_mfma_f32_16x16x32_bf16 v[8:11], v[136:139], v[60:63], v[8:11]\n"
    " v_mfma_f32_16x16x32_bf16 v[16:19], v[140:143], v[60:63], v[16:19]\n"
    " ds_read_b64_tr_b16 v[56:57], v49 offset:12288\n"
    " ds_read_b64_tr_b16 v[58:59], v49 offset:12800\n"
    " s_waitcnt lgkmcnt(8)\n"
    " v_mfma_f32_16x16x32_bf16 v[12:15], v[136:139], v[64:67], v[12:15]\n"
    " v_mfma_f32_16x16x32_bf16 v[20:23], v[140:143], v[64:67], v[20:23]\n"
    " buffer_load_dwordx4 v[80:83], v1, s[12:15], s68 offen offset:1024 sc1\n"
    " ds_read_b64_tr_b16 v[60:61], v48 offset:16384\n"
    " ds_read_b64_tr_b16 v[62:63], v48 offset:16896\n"
    " s_waitcnt lgkmcnt(8)\n"
    " v_mfma_f32_16x16x32_bf16 v[8:11], v[144:147], v[68:71], v[8:11]\n"
    " v_mfma_f32_16x16x32_bf16 v[16:19], v[148:151], v[68:71], v[16:19]\n"
    " ds_read_b64_tr_b16 v[64:65], v49 offset:16384\n"
    " ds_read_b64_tr_b16 v[66:67], v49 offset:16896\n"
    " s_waitcnt lgkmcnt(8)\n"
    " v_mfma_f32_16x16x32_bf16 v[12:15], v[144:147], v[72:75], v[12:15]\n"
    " v_mfma_f32_16x16x32_bf16 v[20:23], v[148:151], v[72:75], v[20:23]\n"
    " buffer_load_dwordx4 v[84:87], v1, s[12:15], s68 offen offset:2048 sc1\n"
    " ds_read_b64_tr_b16 v[68:69], v48 offset:20480\n"
    " ds_read_b64_tr_b16 v[70:71], v48 offset:20992\n"
    " s_waitcnt lgkmcnt(8)\n"
    " v_mfma_f32_16x16x32_bf16 v[8:11], v[152:155], v[52:55], v[8:11]\n"
    " v_mfma_f32_16x16x32_bf16 v[16:19], v[156:159], v[52:55], v[16:19]\n"
    " ds_read_b64_tr_b16 v[72:73], v49 offset:20480\n"
    " ds_read_b64_tr_b16 v[74:75], v49 offset:20992\n"
    " s_waitcnt lgkmcnt(8)\n"
    " v_mfma_f32_16x16x32_bf16 v[12:15], v[152:155], v[56:59], v[12:15]\n"
    " v_mfma_f32_16x16x32_bf16 v[20:23], v[156:159], v[56:59], v[20:23]\n"
    " buffer_load_dwordx4 v[88:91], v1, s[12:15], s68 offen offset:3072 sc1\n"
    " ds_read_b64_tr_b16 v[52:53], v48 offset:24576\n"
    " ds_read_b64_tr_b16 v[54:55], v48 offset:25088\n"
    " s_waitcnt lgkmcnt(8)\n"
    " v_mfma_f32_16x16x32_bf16 v[8:11], v[160:163], v[60:63], v[8:11]\n"
    " v_mfma_f32_16x16x32_bf16 v[16:19], v[164:167], v[60:63], v[16:19]\n"
    " ds_read_b64_tr_b16 v[56:57], v49 offset:24576\n"
    " ds_read_b64_tr_b16 v[58:59], v49 offset:25088\n"
    " s_waitcnt lgkmcnt(8)\n"
    " v_mfma_f32_16x16x32_bf16 v[12:15], v[160:163], v[64:67], v[12:15]\n"
    " v_mfma_f32_16x16x32_bf16 v[20:23], v[164:167], v[64:67], v[20:23]\n"
    " s_add_u32 s68, s68, 4096\n"
    " buffer_load_dwordx4 v[92:95], v1, s[12:15], s68 offen offset:0 sc1\n"
    " ds_read_b64_tr_b16 v[60:61], v48 offset:28672\n"
    " ds_read_b64_tr_b16 v[62:63], v48 offset:29184\n"
    " s_waitcnt lgkmcnt(8)\n"
    " v_mfma_f32_16x16x32_bf16 v[8:11], v[168:171], v[68:71], v[8:11]\n"
    " v_mfma_f32_16x16x32_bf16 v[16:19], v[172:175], v[68:71], v[16:19]\n"
    " ds_read_b64_tr_b16 v[64:65], v49 offset:28672\n"
    " ds_read_b64_tr_b16 v[66:67], v49 offset:29184\n"
    " s_waitcnt lgkmcnt(8)\n"
    " v_mfma_f32_16x16x32_bf16 v[12:15], v[168:171], v[72:75], v[12:15]\n"
    " v_mfma_f32_16x16x32_bf16 v[20:23], v[172:175], v[72:75], v[20:23]\n"
    " buffer_load_dwordx4 v[96:99], v1, s[12:15], s68 offen offset:1024 sc1\n"
    " ds_read_b64_tr_b16 v[68:69], v48 offset:32768\n"
    " ds_read_b64_tr_b16 v[70:71], v48 offset:33280\n"
    " s_waitcnt lgkmcnt(8)\n"
    " v_mfma_f32_16x16x32_bf16 v[8:11], v[176:179], v[52:55], v[8:11]\n"
    " v_mfma_f32_16x16x32_bf16 v[16:19], v[180:183], v[52:55], v[16:19]\n"
    " ds_read_b64_tr_b16 v[72:73], v49 offset:32768\n"
    " ds_read_b64_tr_b16 v[74:75], v49 offset:33280\n"
    " s_waitcnt lgkmcnt(8)\n"
    " v_mfma_f32_16x16x32_bf16 v[12:15], v[176:179], v[56:59], v[12:15]\n"
    " v_mfma_f32_16x16x32_bf16 v[20:23], v[180:183], v[56:59], v[20:23]\n"
    " v_mov_b32 v6, 1.0\n"
    " s_waitcnt vmcnt(12)\n"
    " v_add_f32 v124, v124, v125\n"
    " s_waitcnt vmcnt(10)\n"
    " v_add_f32 v126, v126, v127\n"
    " v_add_f32 v124, v124, v126\n"
    " s_waitcnt vmcnt(8)\n"
    " v_add_f32 v120, v120, v121\n"
    " s_waitcnt vmcnt(6)\n"
    " v_add_f32 v122, v122, v123\n"
    " v_add_f32 v120, v120, v122\n"
    " s_nop 1\n"
    " v_mfma_f32_16x16x4_f32 v[40:43], v124, v6, 0\n"
    " v_mfma_f32_16x16x4_f32 v[44:47], v120, v6, 0\n"
    " buffer_load_dwordx4 v[100:103], v1, s[12:15], s68 offen offset:2048 sc1\n"
    " ds_read_b64_tr_b16 v[52:53], v48 offset:36864\n"
    " ds_read_b64_tr_b16 v[54:55], v48 offset:37376\n"
    " s_waitcnt lgkmcnt(8)\n"
    " v_mfma_f32_16x16x32_bf16 v[8:11], v[184:187], v[60:63], v[8:11]\n"
    " v_mfma_f32_16x16x32_bf16 v[16:19], v[188:191], v[60:63], v[16:19]\n"
    " ds_read_b64_tr_b16 v[56:57], v49 offset:36864\n"
    " ds_read_b64_tr_b16 v[58:59], v49 offset:37376\n"
    " s_waitcnt lgkmcnt(8)\n"
    " v_mfma_f32_16x16x32_bf16 v[12:15], v[184:187], v[64:67], v[12:15]\n"
    " v_mfma_f32_16x16x32_bf16 v[20:23], v[188:191], v[64:67], v[20:23]\n"
    " buffer_load_dwordx4 v[104:107], v1, s[12:15], s68 offen offset:3072 sc1\n"
    " ds_read_b64_tr_b16 v[60:61], v48 offset:40960\n"
    " ds_read_b64_tr_b16 v[62:63], v48 offset:41472\n"
    " s_waitcnt lgkmcnt(8)\n"
    " v_mfma_f32_16x16x32_bf16 v[8:11], v[192:195], v[68:71], v[8:11]\n"
    " v_mfma_f32_16x16x32_bf16 v[16:19], v[196:199], v[68:71], v[16:19]\n"
    " ds_read_b64_tr_b16 v[64:65], v49 offset:40960\n"
    " ds_read_b64_tr_b16 v[66:67], v49 offset:41472\n"
    " s_waitcnt lgkmcnt(8)\n"
    " v_mfma_f32_16x16x32_bf16 v[12:15], v[192:195], v[72:75], v[12:15]\n"
    " v_mfma_f32_16x16x32_bf16 v[20:23], v[196:199], v[72:75], v[20:23]\n"
    " s_add_u32 s68, s68, 4096\n"
    " buffer_load_dwordx4 v[108:111], v1, s[12:15], s68 offen offset:0 sc1\n"
    " ds_read_b64_tr_b16 v[68:69], v48 offset:45056\n"
    " ds_read_b64_tr_b16 v[70:71], v48 offset:45568\n"
    " s_waitcnt lgkmcnt(8)\n"
    " v_mfma_f32_16x16x32_bf16 v[8:11], v[200:203], v[52:55], v[8:11]\n"
    " v_mfma_f32_16x16x32_bf16 v[16:19], v[204:207], v[52:55], v[16:19]\n"
    " ds_read_b64_tr_b16 v[72:73], v49 offset:45056\n"
    " ds_read_b64_tr_b16 v[74:75], v49 offset:45568\n"
    " s_waitcnt lgkmcnt(8)\n"
    " v_mfma_f32_16x16x32_bf16 v[12:15], v[200:203], v[56:59], v[12:15]\n"
    " v_mfma_f32_16x16x32_bf16 v[20:23], v[204:207], v[56:59], v[20:23]\n"
    " buffer_load_dwordx4 v[112:115], v1, s[12:15], s68 offen offset:1024 sc1\n"
    " ds_read_b64_tr_b16 v[52:53], v48 offset:49152\n"
    " ds_read_b64_tr_b16 v[54:55], v48 offset:49664\n"
    " s_waitcnt lgkmcnt(8)\n"
    " v_mfma_f32_16x16x32_bf16 v[8:11], v[208:211], v[60:63], v[8:11]\n"
    " v_mfma_f32_16x16x32_bf16 v[16:19], v[212:215], v[60:63], v[16:19]\n"
    " ds_read_b64_tr_b16 v[56:57], v49 offset:49152\n"
    " ds_read_b64_tr_b16 v[58:59], v49 offset:49664\n"
    " s_waitcnt lgkmcnt(8)\n"
    " v_mfma_f32_16x16x32_bf16 v[12:15], v[208:211], v[64:67], v[12:15]\n"
    " v_mfma_f32_16x16x32_bf16 v[20:23], v[212:215], v[64:67], v[20:23]\n"
    " buffer_load_dwordx4 v[116:119], v1, s[12:15], s68 offen offset:2048 sc1\n"
    " ds_read_b64_tr_b16 v[60:61], v48 offset:53248\n"
    " ds_read_b64_tr_b16 v[62:63], v48 offset:53760\n"
    " s_waitcnt lgkmcnt(8)\n"
    " v_mfma_f32_16x16x32_bf16 v[8:11], v[216:219], v[68:71], v[8:11]\n"
    " v_mfma_f32_16x16x32_bf16 v[16:19], v[220:223], v[68:71], v[16:19]\n"
    " ds_read_b64_tr_b16 v[64:65], v49 offset:53248\n"
    " ds_read_b64_tr_b16 v[66:67], v49 offset:53760\n"
    " s_waitcnt lgkmcnt(8)\n"
    " v_mfma_f32_16x16x32_bf16 v[12:15], v[216:219], v[72:75], v[12:15]\n"
    " v_mfma_f32_16x16x32_bf16 v[20:23], v[220:223], v[72:75], v[20:23]\n"
    " buffer_load_dwordx4 v[124:127], v1, s[12:15], s68 offen offset:3072 sc1\n"
    " ds_read_b64_tr_b16 v[68:69], v48 offset:57344\n"
    " ds_read_b64_tr_b16 v[70:71], v48 offset:57856\n"
    " s_waitcnt lgkmcnt(8)\n"
    " v_mfma_f32_16x16x32_bf16 v[8:11], v[224:227], v[52:55], v[8:11]\n"
    " v_mfma_f32_16x16x32_bf16 v[16:19], v[228:231], v[52:55], v[16:19]\n"
    " ds_read_b64_tr_b16 v[72:73], v49 offset:57344\n"
    " ds_read_b64_tr_b16 v[74:75], v49 offset:57856\n"
    " s_waitcnt lgkmcnt(8)\n"
    " v_mfma_f32_16x16x32_bf16 v[12:15], v[224:227], v[56:59], v[12:15]\n"
    " v_mfma_f32_16x16x32_bf16 v[20:23], v[228:231], v[56:59], v[20:23]\n"
    " s_add_u32 s68, s68, 4096\n"
    " buffer_load_dwordx4 v[120:123], v1, s[12:15], s68 offen offset:0 sc1\n"
    " ds_read_b64_tr_b16 v[52:53], v48 offset:61440\n"
    " ds_read_b64_tr_b16 v[54:55], v48 offset:61952\n"
    " s_waitcnt lgkmcnt(8)\n"
    " v_mfma_f32_16x16x32_bf16 v[8:11], v[232:235], v[60:63], v[8:11]\n"
    " v_mfma_f32_16x16x32_bf16 v[16:19], v[236:239], v[60:63], v[16:19]\n"
    " ds_read_b64_tr_b16 v[56:57], v49 offset:61440\n"
    " ds_read_b64_tr_b16 v[58:59], v49 offset:61952\n"
    " s_waitcnt lgkmcnt(8)\n"
    " v_mfma_f32_16x16x32_bf16 v[12:15], v[232:235], v[64:67], v[12:15]\n"
    " v_mfma_f32_16x16x32_bf16 v[20:23], v[236:239], v[64:67], v[20:23]\n"
    " ds_read_b64_tr_b16 v[60:61], v50 offset:0\n"
    " ds_read_b64_tr_b16 v[62:63], v50 offset:512\n"
    " s_waitcnt lgkmcnt(8)\n"
    " v_mfma_f32_16x16x32_bf16 v[8:11], v[240:243], v[68:71], v[8:11]\n"
    " v_mfma_f32_16x16x32_bf16 v[16:19], v[244:247], v[68:71], v[16:19]\n"
    " ds_read_b64_tr_b16 v[64:65], v51 offset:0\n"
    " ds_read_b64_tr_b16 v[66:67], v51 offset:512\n"
    " s_waitcnt lgkmcnt(8)\n"
    " v_mfma_f32_16x16x32_bf16 v[12:15], v[240:243], v[72:75], v[12:15]\n"
    " v_mfma_f32_16x16x32_bf16 v[20:23], v[244:247], v[72:75], v[20:23]\n"
    " ds_read_b64_tr_b16 v[68:69], v50 offset:4096\n"
    " ds_read_b64_tr_b16 v[70:71], v50 offset:4608\n"
    " s_waitcnt lgkmcnt(8)\n"
    " v_mfma_f32_16x16x32_bf16 v[8:11], v[248:251], v[52:55], v[8:11]\n"
    " v_mfma_f32_16x16x32_bf16 v[16:19], v[252:255], v[52:55], v[16:19]\n"
    " ds_read_b64_tr_b16 v[72:73], v51 offset:4096\n"
    " ds_read_b64_tr_b16 v[74:75], v51 offset:4608\n"
    " s_waitcnt lgkmcnt(8)\n"
    " v_mfma_f32_16x16x32_bf16 v[12:15], v[248:251], v[56:59], v[12:15]\n"
    " v_mfma_f32_16x16x32_bf16 v[20:23], v[252:255], v[56:59], v[20:23]\n"
    " s_barrier\n"
    " ds_read_b64_tr_b16 v[52:53], v50 offset:8192\n"
    " ds_read_b64_tr_b16 v[54:55], v50 offset:8704\n"
    " s_waitcnt lgkmcnt(8)\n"
    " v_mfma_f32_16x16x32_bf16 v[24:27], v[128:131], v[60:63], 0\n"
    " ds_read_b64 v[2:3], v7 offset:0\n"
    " s_waitcnt lgkmcnt(0)\n"
    " v_mul_f32 v8, v8, v3\n"
    " v_mul_f32 v9, v9, v3\n"
    " v_mul_f32 v10, v10, v3\n"
    " v_mul_f32 v11, v11, v3\n"
    " v_mfma_f32_16x16x32_bf16 v[32:35], v[132:135], v[60:63], 0\n"
    " v_fmac_f32 v8, v40, v2\n"
    " v_fmac_f32 v9, v41, v2\n"
    " v_fmac_f32 v10, v42, v2\n"
    " v_fmac_f32 v11, v43, v2\n"
    " v_mul_f32 v16, v16, v3\n"
    " v_mul_f32 v17, v17, v3\n"
    " v_mul_f32 v18, v18, v3\n"
    " v_mul_f32 v19, v19, v3\n"
    " ds_read_b64_tr_b16 v[56:57], v51 offset:8192\n"
    " ds_read_b64_tr_b16 v[58:59], v51 offset:8704\n"
    " v_mfma_f32_16x16x32_bf16 v[28:31], v[128:131], v[64:67], 0\n"
    " v_fmac_f32 v16, v44, v2\n"
    " v_fmac_f32 v17, v45, v2\n"
    " v_fmac_f32 v18, v46, v2\n"
    " v_fmac_f32 v19, v47, v2\n"
    " ds_read_b64 v[2:3], v7 offset:128\n"
    " v_mfma_f32_16x16x32_bf16 v[36:39], v[132:135], v[64:67], 0\n"
    " v_cvt_pk_bf16_f32 v8, v8, v9\n"
    " v_cvt_pk_bf16_f32 v9, v10, v11\n"
    " v_cvt_pk_bf16_f32 v10, v16, v17\n"
    " v_cvt_pk_bf16_f32 v11, v18, v19\n"
    " buffer_load_dwordx4 v[128:131], v1, s[12:15], s68 offen offset:1024 sc1\n"
    " ds_read_b64_tr_b16 v[60:61], v50 offset:12288\n"
    " ds_read_b64_tr_b16 v[62:63], v50 offset:12800\n"
    " v_mfma_f32_16x16x32_bf16 v[24:27], v[136:139], v[68:71], v[24:27]\n"
    " v_pk_max_i16 v8, v8, 0\n"
    " v_pk_max_i16 v9, v9, 0\n"
    " v_pk_max_i16 v10, v10, 0\n"
    " v_pk_max_i16 v11, v11, 0\n"
    " v_add_u32 v5, s70, v1\n"
    " v_mfma_f32_16x16x32_bf16 v[32:35], v[140:143], v[68:71], v[32:35]\n"
    " v_or_b32 v8, s88, v8\n"
    " v_or_b32 v9, s89, v9\n"
    " v_or_b32 v10, s89, v10\n"
    " v_or_b32 v11, s88, v11\n"
    " ds_read_b64_tr_b16 v[64:65], v51 offset:12288\n"
    " ds_read_b64_tr_b16 v[66:67], v51 offset:12800\n"
    " v_mfma_f32_16x16x32_bf16 v[28:31], v[136:139], v[72:75], v[28:31]\n"
    " buffer_store_dwordx4 v[8:11], v5, s[12:15], 0 offen sc1\n"
    " s_add_u32 s70, s70, 57344\n"
    " s_waitcnt lgkmcnt(4)\n"
    " v_mul_f32 v12, v12, v3\n"
    " v_mul_f32 v13, v13, v3\n"
    " v_mul_f32 v14, v14, v3\n"
    " v_mul_f32 v15, v15, v3\n"
    " v_mfma_f32_16x16x32_bf16 v[36:39], v[140:143], v[72:75], v[36:39]\n"
    " v_fmac_f32 v12, v40, v2\n"
    " v_fmac_f32 v13, v41, v2\n"
    " v_fmac_f32 v14, v42, v2\n"
    " v_fmac_f32 v15, v43, v2\n"
    " v_mul_f32 v20, v20, v3\n"
    " v_mul_f32 v21, v21, v3\n"
    " v_mul_f32 v22, v22, v3\n"
    " v_mul_f32 v23, v23, v3\n"
    " buffer_load_dwordx4 v[132:135], v1, s[12:15], s68 offen offset:2048 sc1\n"
    " buffer_load_dwordx4 v[136:139], v1, s[12:15], s68 offen offset:3072 sc1\n"
    " ds_read_b64_tr_b16 v[68:69], v50 offset:16384\n"
    " ds_read_b64_tr_b16 v[70:71], v50 offset:16896\n"
    " v_mfma_f32_16x16x32_bf16 v[24:27], v[144:147], v[52:55], v[24:27]\n"
    " v_fmac_f32 v20, v44, v2\n"
    " v_fmac_f32 v21, v45, v2\n"
    " v_fmac_f32 v22, v46, v2\n"
    " v_fmac_f32 v23, v47, v2\n"
    " v_cvt_pk_bf16_f32 v12, v12, v13\n"
    " v_cvt_pk_bf16_f32 v13, v14, v15\n"
    " v_mfma_f32_16x16x32_bf16 v[32:35], v[148:151], v[52:55], v[32:35]\n"
    " v_cvt_pk_bf16_f32 v14, v20, v21\n"
    " v_cvt_pk_bf16_f32 v15, v22, v23\n"
    " v_pk_max_i16 v12, v12, 0\n"
    " v_pk_max_i16 v13, v13, 0\n"
    " ds_read_b64_tr_b16 v[72:73], v51 offset:16384\n"
    " ds_read_b64_tr_b16 v[74:75], v51 offset:16896\n"
    " v_mfma_f32_16x16x32_bf16 v[28:31], v[144:147], v[56:59], v[28:31]\n"
    " v_pk_max_i16 v14, v14, 0\n"
    " v_pk_max_i16 v15, v15, 0\n"
    " v_add_u32 v5, s70, v1\n"
    " v_or_b32 v12, s88, v12\n"
    " v_or_b32 v13, s89, v13\n"
    " v_mfma_f32_16x16x32_bf16 v[36:39], v[148:151], v[56:59], v[36:39]\n"
    " v_or_b32 v14, s89, v14\n"
    " v_or_b32 v15, s88, v15\n"
    " buffer_store_dwordx4 v[12:15], v5, s[12:15], 0 offen sc1\n"
    " s_add_u32 s70, s70, 57344\n"
    " s_add_u32 s68, s68, 4096\n"
    " buffer_load_dwordx4 v[140:143], v1, s[12:15], s68 offen offset:0 sc1\n"
    " ds_read_b64_tr_b16 v[52:53], v50 offset:20480\n"
    " ds_read_b64_tr_b16 v[54:55], v50 offset:20992\n"
    " s_waitcnt lgkmcnt(8)\n"
    " v_mfma_f32_16x16x32_bf16 v[24:27], v[152:155], v[60:63], v[24:27]\n"
    " v_mfma_f32_16x16x32_bf16 v[32:35], v[156:159], v[60:63], v[32:35]\n"
    " ds_read_b64_tr_b16 v[56:57], v51 offset:20480\n"
    " ds_read_b64_tr_b16 v[58:59], v51 offset:20992\n"
    " s_waitcnt lgkmcnt(8)\n"
    " v_mfma_f32_16x16x32_bf16 v[28:31], v[152:155], v[64:67], v[28:31]\n"
    " v_mfma_f32_16x16x32_bf16 v[36:39], v[156:159], v[64:67], v[36:39]\n"
    " buffer_load_dwordx4 v[8:11], v1, s[12:15], s68 offen offset:1024 sc1\n"
    " buffer_load_dwordx4 v[12:15], v1, s[12:15], s68 offen offset:2048 sc1\n"
    " ds_read_b64_tr_b16 v[60:61], v50 offset:24576\n"
    " ds_read_b64_tr_b16 v[62:63], v50 offset:25088\n"
    " s_waitcnt lgkmcnt(8)\n"
    " v_mfma_f32_16x16x32_bf16 v[24:27], v[160:163], v[68:71], v[24:27]\n"
    " v_mfma_f32_16x16x32_bf16 v[32:35], v[164:167], v[68:71], v[32:35]\n"
    " ds_read_b64_tr_b16 v[64:65], v51 offset:24576\n"
    " ds_read_b64_tr_b16 v[66:67], v51 offset:25088\n"
    " s_waitcnt lgkmcnt(8)\n"
    " v_mfma_f32_16x16x32_bf16 v[28:31], v[160:163], v[72:75], v[28:31]\n"
    " v_mfma_f32_16x16x32_bf16 v[36:39], v[164:167], v[72:75], v[36:39]\n"
    " buffer_load_dwordx4 v[16:19], v1, s[12:15], s68 offen offset:3072 sc1\n"
    " ds_read_b64_tr_b16 v[68:69], v50 offset:28672\n"
    " ds_read_b64_tr_b16 v[70:71], v50 offset:29184\n"
    " s_waitcnt lgkmcnt(8)\n"
    " v_mfma_f32_16x16x32_bf16 v[24:27], v[168:171], v[52:55], v[24:27]\n"
    " v_mfma_f32_16x16x32_bf16 v[32:35], v[172:175], v[52:55], v[32:35]\n"
    " ds_read_b64_tr_b16 v[72:73], v51 offset:28672\n"
    " ds_read_b64_tr_b16 v[74:75], v51 offset:29184\n"
    " s_waitcnt lgkmcnt(8)\n"
    " v_mfma_f32_16x16x32_bf16 v[28:31], v[168:171], v[56:59], v[28:31]\n"
    " v_mfma_f32_16x16x32_bf16 v[36:39], v[172:175], v[56:59], v[36:39]\n"
    " s_add_u32 s68, s68, 4096\n"
    " buffer_load_dwordx4 v[20:23], v1, s[12:15], s68 offen offset:0 sc1\n"
    " buffer_load_dwordx4 v[144:147], v1, s[12:15], s68 offen offset:1024 sc1\n"
    " ds_read_b64_tr_b16 v[52:53], v50 offset:32768\n"
    " ds_read_b64_tr_b16 v[54:55], v50 offset:33280\n"
    " s_waitcnt lgkmcnt(8)\n"
    " v_mfma_f32_16x16x32_bf16 v[24:27], v[176:179], v[60:63], v[24:27]\n"
    " v_mfma_f32_16x16x32_bf16 v[32:35], v[180:183], v[60:63], v[32:35]\n"
    " ds_read_b64_tr_b16 v[56:57], v51 offset:32768\n"
    " ds_read_b64_tr_b16 v[58:59], v51 offset:33280\n"
    " s_waitcnt lgkmcnt(8)\n"
    " v_mfma_f32_16x16x32_bf16 v[28:31], v[176:179], v[64:67], v[28:31]\n"
    " v_mfma_f32_16x16x32_bf16 v[36:39], v[180:183], v[64:67], v[36:39]\n"
    " buffer_load_dwordx4 v[148:151], v1, s[12:15], s68 offen offset:2048 sc1\n"
    " ds_read_b64_tr_b16 v[60:61], v50 offset:36864\n"
    " ds_read_b64_tr_b16 v[62:63], v50 offset:37376\n"
    " s_waitcnt lgkmcnt(8)\n"
    " v_mfma_f32_16x16x32_bf16 v[24:27], v[184:187], v[68:71], v[24:27]\n"
    " v_mfma_f32_16x16x32_bf16 v[32:35], v[188:191], v[68:71], v[32:35]\n"
    " ds_read_b64_tr_b16 v[64:65], v51 offset:36864\n"
    " ds_read_b64_tr_b16 v[66:67], v51 offset:37376\n"
    " s_waitcnt lgkmcnt(8)\n"
    " v_mfma_f32_16x16x32_bf16 v[28:31], v[184:187], v[72:75], v[28:31]\n"
    " v_mfma_f32_16x16x32_bf16 v[36:39], v[188:191], v[72:75], v[36:39]\n"
    " buffer_load_dwordx4 v[152:155], v1, s[12:15], s68 offen offset:3072 sc1\n"
    " s_add_u32 s68, s68, 4096\n"
    " buffer_load_dwordx4 v[156:159], v1, s[12:15], s68 offen offset:0 sc1\n"
    " ds_read_b64_tr_b16 v[68:69], v50 offset:40960\n"
    " ds_read_b64_tr_b16 v[70:71], v50 offset:41472\n"
    " s_waitcnt lgkmcnt(8)\n"
    " v_mfma_f32_16x16x32_bf16 v[24:27], v[192:195], v[52:55], v[24:27]\n"
    " v_mfma_f32_16x16x32_bf16 v[32:35], v[196:199], v[52:55], v[32:35]\n"
    " ds_read_b64_tr_b16 v[72:73], v51 offset:40960\n"
    " ds_read_b64_tr_b16 v[74:75], v51 offset:41472\n"
    " s_waitcnt lgkmcnt(8)\n"
    " v_mfma_f32_16x16x32_bf16 v[28:31], v[192:195], v[56:59], v[28:31]\n"
    " v_mfma_f32_16x16x32_bf16 v[36:39], v[196:199], v[56:59], v[36:39]\n"
    " buffer_load_dwordx4 v[160:163], v1, s[12:15], s68 offen offset:1024 sc1\n"
    " ds_read_b64_tr_b16 v[52:53], v50 offset:45056\n"
    " ds_read_b64_tr_b16 v[54:55], v50 offset:45568\n"
    " s_waitcnt lgkmcnt(8)\n"
    " v_mfma_f32_16x16x32_bf16 v[24:27], v[200:203], v[60:63], v[24:27]\n"
    " v_mfma_f32_16x16x32_bf16 v[32:35], v[204:207], v[60:63], v[32:35]\n"
    " ds_read_b64_tr_b16 v[56:57], v51 offset:45056\n"
    " ds_read_b64_tr_b16 v[58:59], v51 offset:45568\n"
    " s_waitcnt lgkmcnt(8)\n"
    " v_mfma_f32_16x16x32_bf16 v[28:31], v[200:203], v[64:67], v[28:31]\n"
    " v_mfma_f32_16x16x32_bf16 v[36:39], v[204:207], v[64:67], v[36:39]\n"
    " buffer_load_dwordx4 v[164:167], v1, s[12:15], s68 offen offset:2048 sc1\n"
    " buffer_load_dwordx4 v[168:171], v1, s[12:15], s68 offen offset:3072 sc1\n"
    " ds_read_b64_tr_b16 v[60:61], v50 offset:49152\n"
    " ds_read_b64_tr_b16 v[62:63], v50 offset:49664\n"
    " s_waitcnt lgkmcnt(8)\n"
    " v_mfma_f32_16x16x32_bf16 v[24:27], v[208:211], v[68:71], v[24:27]\n"
    " v_mfma_f32_16x16x32_bf16 v[32:35], v[212:215], v[68:71], v[32:35]\n"
    " ds_read_b64_tr_b16 v[64:65], v51 offset:49152\n"
    " ds_read_b64_tr_b16 v[66:67], v51 offset:49664\n"
    " s_waitcnt lgkmcnt(8)\n"
    " v_mfma_f32_16x16x32_bf16 v[28:31], v[208:211], v[72:75], v[28:31]\n"
    " v_mfma_f32_16x16x32_bf16 v[36:39], v[212:215], v[72:75], v[36:39]\n"
    " buffer_load_dwordx4 v[172:175], v1, s[12:15], s72 offen offset:0 sc1\n"
    " ds_read_b64_tr_b16 v[68:69], v50 offset:53248\n"
    " ds_read_b64_tr_b16 v[70:71], v50 offset:53760\n"
    " s_waitcnt lgkmcnt(8)\n"
    " v_mfma_f32_16x16x32_bf16 v[24:27], v[216:219], v[52:55], v[24:27]\n"
    " v_mfma_f32_16x16x32_bf16 v[32:35], v[220:223], v[52:55], v[32:35]\n"
    " ds_read_b64_tr_b16 v[72:73], v51 offset:53248\n"
    " ds_read_b64_tr_b16 v[74:75], v51 offset:53760\n"
    " s_waitcnt lgkmcnt(8)\n"
    " v_mfma_f32_16x16x32_bf16 v[28:31], v[216:219], v[56:59], v[28:31]\n"
    " v_mfma_f32_16x16x32_bf16 v[36:39], v[220:223], v[56:59], v[36:39]\n"
    " buffer_load_dwordx4 v[176:179], v1, s[12:15], s72 offen offset:1024 sc1\n"
    " buffer_load_dwordx4 v[180:183], v1, s[12:15], s72 offen offset:2048 sc1\n"
    " ds_read_b64_tr_b16 v[52:53], v50 offset:57344\n"
    " ds_read_b64_tr_b16 v[54:55], v50 offset:57856\n"
    " s_waitcnt lgkmcnt(8)\n"
    " v_mfma_f32_16x16x32_bf16 v[24:27], v[224:227], v[60:63], v[24:27]\n"
    " v_mfma_f32_16x16x32_bf16 v[32:35], v[228:231], v[60:63], v[32:35]\n"
    " ds_read_b64_tr_b16 v[56:57], v51 offset:57344\n"
    " ds_read_b64_tr_b16 v[58:59], v51 offset:57856\n"
    " s_waitcnt lgkmcnt(8)\n"
    " v_mfma_f32_16x16x32_bf16 v[28:31], v[224:227], v[64:67], v[28:31]\n"
    " v_mfma_f32_16x16x32_bf16 v[36:39], v[228:231], v[64:67], v[36:39]\n"
    " buffer_load_dwordx4 v[184:187], v1, s[12:15], s72 offen offset:3072 sc1\n"
    " ds_read_b64_tr_b16 v[60:61], v50 offset:61440\n"
    " ds_read_b64_tr_b16 v[62:63], v50 offset:61952\n"
    " s_waitcnt lgkmcnt(8)\n"
    " v_mfma_f32_16x16x32_bf16 v[24:27], v[232:235], v[68:71], v[24:27]\n"
    " v_mfma_f32_16x16x32_bf16 v[32:35], v[236:239], v[68:71], v[32:35]\n"
    " ds_read_b64_tr_b16 v[64:65], v51 offset:61440\n"
    " ds_read_b64_tr_b16 v[66:67], v51 offset:61952\n"
    " s_waitcnt lgkmcnt(8)\n"
    " v_mfma_f32_16x16x32_bf16 v[28:31], v[232:235], v[72:75], v[28:31]\n"
    " v_mfma_f32_16x16x32_bf16 v[36:39], v[236:239], v[72:75], v[36:39]\n"
    " buffer_load_dwordx4 v[188:191], v1, s[12:15], s73 offen offset:0 sc1\n"
    " buffer_load_dwordx4 v[192:195], v1, s[12:15], s73 offen offset:1024 sc1\n"
    " s_waitcnt lgkmcnt(6)\n"
    " v_mfma_f32_16x16x32_bf16 v[24:27], v[240:243], v[52:55], v[24:27]\n"
    " v_mfma_f32_16x16x32_bf16 v[32:35], v[244:247], v[52:55], v[32:35]\n"
    " s_waitcnt lgkmcnt(4)\n"
    " v_mfma_f32_16x16x32_bf16 v[28:31], v[240:243], v[56:59], v[28:31]\n"
    " v_mfma_f32_16x16x32_bf16 v[36:39], v[244:247], v[56:59], v[36:39]\n"
    " buffer_load_dwordx4 v[196:199], v1, s[12:15], s73 offen offset:2048 sc1\n"
    " s_waitcnt lgkmcnt(2)\n"
    " v_mfma_f32_16x16x32_bf16 v[24:27], v[248:251], v[60:63], v[24:27]\n"
    " v_mfma_f32_16x16x32_bf16 v[32:35], v[252:255], v[60:63], v[32:35]\n"
    " s_waitcnt lgkmcnt(0)\n"
    " v_mfma_f32_16x16x32_bf16 v[28:31], v[248:251], v[64:67], v[28:31]\n"
    " v_mfma_f32_16x16x32_bf16 v[36:39], v[252:255], v[64:67], v[36:39]\n"
    " s_waitcnt lgkmcnt(0)\n"
    " s_barrier\n"
    " ds_read_b64 v[2:3], v7 offset:256\n"
    " s_waitcnt lgkmcnt(0)\n"
    " v_mul_f32 v24, v24, v3\n"
    " v_mul_f32 v25, v25, v3\n"
    " v_mul_f32 v26, v26, v3\n"
    " v_mul_f32 v27, v27, v3\n"
    " v_fmac_f32 v24, v40, v2\n"
    " v_fmac_f32 v25, v41, v2\n"
    " v_fmac_f32 v26, v42, v2\n"
    " v_fmac_f32 v27, v43, v2\n"
    " v_mul_f32 v32, v32, v3\n"
    " v_mul_f32 v33, v33, v3\n"
    " v_mul_f32 v34, v34, v3\n"
    " v_mul_f32 v35, v35, v3\n"
    " v_fmac_f32 v32, v44, v2\n"
    " v_fmac_f32 v33, v45, v2\n"
    " v_fmac_f32 v34, v46, v2\n"
    " v_fmac_f32 v35, v47, v2\n"
    " ds_read_b64 v[2:3], v7 offset:384\n"
    " v_cvt_pk_bf16_f32 v24, v24, v25\n"
    " v_cvt_pk_bf16_f32 v25, v26, v27\n"
    " v_cvt_pk_bf16_f32 v26, v32, v33\n"
    " v_cvt_pk_bf16_f32 v27, v34, v35\n"
    " v_pk_max_i16 v24, v24, 0\n"
    " v_pk_max_i16 v25, v25, 0\n"
    " v_pk_max_i16 v26, v26, 0\n"
    " v_pk_max_i16 v27, v27, 0\n"
    " v_add_u32 v5, s70, v1\n"
    " v_or_b32 v24, s88, v24\n"
    " v_or_b32 v25, s89, v25\n"
    " v_or_b32 v26, s89, v26\n"
    " v_or_b32 v27, s88, v27\n"
    " buffer_store_dwordx4 v[24:27], v5, s[12:15], 0 offen sc1\n"
    " s_add_u32 s70, s70, 57344\n"
    " s_waitcnt lgkmcnt(0)\n"
    " v_mul_f32 v28, v28, v3\n"
    " v_mul_f32 v29, v29, v3\n"
    " v_mul_f32 v30, v30, v3\n"
    " v_mul_f32 v31, v31, v3\n"
    " v_fmac_f32 v28, v40, v2\n"
    " v_fmac_f32 v29, v41, v2\n"
    " v_fmac_f32 v30, v42, v2\n"
    " v_fmac_f32 v31, v43, v2\n"
    " v_mul_f32 v36, v36, v3\n"
    " v_mul_f32 v37, v37, v3\n"
    " v_mul_f32 v38, v38, v3\n"
    " v_mul_f32 v39, v39, v3\n"
    " v_fmac_f32 v36, v44, v2\n"
    " v_fmac_f32 v37, v45, v2\n"
    " v_fmac_f32 v38, v46, v2\n"
    " v_fmac_f32 v39, v47, v2\n"
    " v_cvt_pk_bf16_f32 v28, v28, v29\n"
    " v_cvt_pk_bf16_f32 v29, v30, v31\n"
    " v_cvt_pk_bf16_f32 v30, v36, v37\n"
    " v_cvt_pk_bf16_f32 v31, v38, v39\n"
    " v_pk_max_i16 v28, v28, 0\n"
    " v_pk_max_i16 v29, v29, 0\n"
    " v_pk_max_i16 v30, v30, 0\n"
    " v_pk_max_i16 v31, v31, 0\n"
    " v_add_u32 v5, s70, v1\n"
    " v_or_b32 v28, s88, v28\n"
    " v_or_b32 v29, s89, v29\n"
    " v_or_b32 v30, s89, v30\n"
    " v_or_b32 v31, s88, v31\n"
    " buffer_store_dwordx4 v[28:31], v5, s[12:15], 0 offen sc1\n"
    " s_add_u32 s70, s70, 57344\n"
    " s_lshl_b32 s60, s44, 13\n"
    " s_add_u32 s60, s60, 66560\n"
    " v_lshrrev_b32 v5, 4, v1\n"
    " v_and_b32 v5, 32, v5\n"
    " v_xor_b32 v5, v5, v1\n"
    " v_add_u32 v48, s60, v5\n"
    " v_xor_b32 v49, 64, v48\n"
    " v_xor_b32 v50, 128, v48\n"
    " v_xor_b32 v51, 192, v48\n"
    " s_waitcnt vmcnt(8)\n"
    " v_bitop3_b32 v2, v172, v175, s88 bitop3:0x7e\n"
    " v_bitop3_b32 v3, v173, v174, s89 bitop3:0x7e\n"
    " v_bitop3_b32 v2, v2, v3, s90 bitop3:0xa8\n"
    " v_cmp_ne_u32 vcc, 0, v2\n"
    " s_cbranch_vccnz .Lgs_118\n"
    ".Lgsb_119:\n"
    " buffer_load_dwordx4 v[232:235], v1, s[12:15], s74 offen offset:0 sc1\n"
    " v_mfma_f32_16x16x32_bf16 v[200:203], v[76:79], v[172:175], 0\n"
    " v_mfma_f32_16x16x32_bf16 v[204:207], v[80:83], v[172:175], 0\n"
    " s_waitcnt vmcnt(8)\n"
    " v_bitop3_b32 v2, v176, v179, s88 bitop3:0x7e\n"
    " v_mfma_f32_16x16x32_bf16 v[208:211], v[84:87], v[172:175], 0\n"
    " v_bitop3_b32 v3, v177, v178, s89 bitop3:0x7e\n"
    " v_mfma_f32_16x16x32_bf16 v[212:215], v[88:91], v[172:175], 0\n"
    " v_bitop3_b32 v2, v2, v3, s90 bitop3:0xa8\n"
    " v_cmp_ne_u32 vcc, 0, v2\n"
    " s_cbranch_vccnz .Lgs_121\n"
    ".Lgsb_122:\n"
    " buffer_load_dwordx4 v[236:239], v1, s[12:15], s74 offen offset:1024 sc1\n"
    " v_mfma_f32_16x16x32_bf16 v[200:203], v[92:95], v[176:179], v[200:203]\n"
    " v_mfma_f32_16x16x32_bf16 v[204:207], v[96:99], v[176:179], v[204:207]\n"
    " s_waitcnt vmcnt(8)\n"
    " v_bitop3_b32 v2, v180, v183, s88 bitop3:0x7e\n"
    " v_mfma_f32_16x16x32_bf16 v[208:211], v[100:103], v[176:179], v[208:211]\n"
    " v_bitop3_b32 v3, v181, v182, s89 bitop3:0x7e\n"
    " v_mfma_f32_16x16x32_bf16 v[212:215], v[104:107], v[176:179], v[212:215]\n"
    " v_bitop3_b32 v2, v2, v3, s90 bitop3:0xa8\n"
    " v_cmp_ne_u32 vcc, 0, v2\n"
    " s_cbranch_vccnz .Lgs_124\n"
    ".Lgsb_125:\n"
    " buffer_load_dwordx4 v[240:243], v1, s[12:15], s74 offen offset:2048 sc1\n"
    " v_mfma_f32_16x16x32_bf16 v[200:203], v[108:111], v[180:183], v[200:203]\n"
    " v_mfma_f32_16x16x32_bf16 v[204:207], v[112:115], v[180:183], v[204:207]\n"
    " s_waitcnt vmcnt(8)\n"
    " v_bitop3_b32 v2, v184, v187, s88 bitop3:0x7e\n"
    " v_mfma_f32_16x16x32_bf16 v[208:211], v[116:119], v[180:183], v[208:211]\n"
    " v_bitop3_b32 v3, v185, v186, s89 bitop3:0x7e\n"
    " v_mfma_f32_16x16x32_bf16 v[212:215], v[124:127], v[180:183], v[212:215]\n"
    " v_bitop3_b32 v2, v2, v3, s90 bitop3:0xa8\n"
    " v_cmp_ne_u32 vcc, 0, v2\n"
    " s_cbranch_vccnz .Lgs_127\n"
    ".Lgsb_128:\n"
    " buffer_load_dwordx4 v[244:247], v1, s[12:15], s74 offen offset:3072 sc1\n"
    " v_mfma_f32_16x16x32_bf16 v[200:203], v[120:123], v[184:187], v[200:203]\n"
    " v_mfma_f32_16x16x32_bf16 v[204:207], v[128:131], v[184:187], v[204:207]\n"
    " s_waitcnt vmcnt(8)\n"
    " v_bitop3_b32 v2, v188, v191, s88 bitop3:0x7e\n"
    " v_mfma_f32_16x16x32_bf16 v[208:211], v[132:135], v[184:187], v[208:211]\n"
    " v_bitop3_b32 v3, v189, v190, s89 bitop3:0x7e\n"
    " v_mfma_f32_16x16x32_bf16 v[212:215], v[136:139], v[184:187], v[212:215]\n"
    " v_bitop3_b32 v2, v2, v3, s90 bitop3:0xa8\n"
    " v_cmp_ne_u32 vcc, 0, v2\n"
    " s_cbranch_vccnz .Lgs_130\n"
    ".Lgsb_131:\n"
    " buffer_load_dwordx4 v[248:251], v1, s[12:15], s75 offen offset:0 sc1\n"
    " v_mfma_f32_16x16x32_bf16 v[200:203], v[140:143], v[188:191], v[200:203]\n"
    " v_mfma_f32_16x16x32_bf16 v[204:207], v[8:11], v[188:191], v[204:207]\n"
    " s_waitcnt vmcnt(8)\n"
    " v_bitop3_b32 v2, v192, v195, s88 bitop3:0x7e\n"
    " v_mfma_f32_16x16x32_bf16 v[208:211], v[12:15], v[188:191], v[208:211]\n"
    " v_bitop3_b32 v3, v193, v194, s89 bitop3:0x7e\n"
    " v_mfma_f32_16x16x32_bf16 v[212:215], v[16:19], v[188:191], v[212:215]\n"
    " v_bitop3_b32 v2, v2, v3, s90 bitop3:0xa8\n"
    " v_cmp_ne_u32 vcc, 0, v2\n"
    " s_cbranch_vccnz .Lgs_133\n"
    ".Lgsb_134:\n"
    " buffer_load_dwordx4 v[252:255], v1, s[12:15], s75 offen offset:1024 sc1\n"
    " v_mfma_f32_16x16x32_bf16 v[200:203], v[20:23], v[192:195], v[200:203]\n"
    " v_mfma_f32_16x16x32_bf16 v[204:207], v[144:147], v[192:195], v[204:207]\n"
    " s_waitcnt vmcnt(8)\n"
    " v_bitop3_b32 v2, v196, v199, s88 bitop3:0x7e\n"
    " v_mfma_f32_16x16x32_bf16 v[208:211], v[148:151], v[192:195], v[208:211]\n"
    " v_bitop3_b32 v3, v197, v198, s89 bitop3:0x7e\n"
    " v_mfma_f32_16x16x32_bf16 v[212:215], v[152:155], v[192:195], v[212:215]\n"
    " v_bitop3_b32 v2, v2, v3, s90 bitop3:0xa8\n"
    " v_cmp_ne_u32 vcc, 0, v2\n"
    " s_cbranch_vccnz .Lgs_136\n"
    ".Lgsb_137:\n"
    " buffer_load_dwordx4 v[52:55], v1, s[12:15], s75 offen offset:2048 sc1\n"
    " v_mfma_f32_16x16x32_bf16 v[200:203], v[156:159], v[196:199], v[200:203]\n"
    " v_mfma_f32_16x16x32_bf16 v[204:207], v[160:163], v[196:199], v[204:207]\n"
    " s_waitcnt vmcnt(6)\n"
    " v_bitop3_b32 v2, v232, v235, s88 bitop3:0x7e\n"
    " v_mfma_f32_16x16x32_bf16 v[208:211], v[164:167], v[196:199], v[208:211]\n"
    " v_bitop3_b32 v3, v233, v234, s89 bitop3:0x7e\n"
    " v_mfma_f32_16x16x32_bf16 v[212:215], v[168:171], v[196:199], v[212:215]\n"
    " v_bitop3_b32 v2, v2, v3, s90 bitop3:0xa8\n"
    " v_cmp_ne_u32 vcc, 0, v2\n"
    " s_cbranch_vccnz .Lgs_139\n"
    ".Lgsb_140:\n"
    " buffer_load_dwordx4 v[56:59], v1, s[12:15], s76 offen offset:0 sc1\n"
    " v_mfma_f32_16x16x32_bf16 v[216:219], v[76:79], v[232:235], 0\n"
    " v_mfma_f32_16x16x32_bf16 v[220:223], v[80:83], v[232:235], 0\n"
    " s_waitcnt vmcnt(6)\n"
    " v_bitop3_b32 v2, v236, v239, s88 bitop3:0x7e\n"
    " v_mfma_f32_16x16x32_bf16 v[224:227], v[84:87], v[232:235], 0\n"
    " v_bitop3_b32 v3, v237, v238, s89 bitop3:0x7e\n"
    " v_mfma_f32_16x16x32_bf16 v[228:231], v[88:91], v[232:235], 0\n"
    " v_bitop3_b32 v2, v2, v3, s90 bitop3:0xa8\n"
    " v_cmp_ne_u32 vcc, 0, v2\n"
    " s_cbranch_vccnz .Lgs_142\n"
    ".Lgsb_143:\n"
    " buffer_load_dwordx4 v[60:63], v1, s[12:15], s76 offen offset:1024 sc1\n"
    " v_mfma_f32_16x16x32_bf16 v[216:219], v[92:95], v[236:239], v[216:219]\n"
    " v_mfma_f32_16x16x32_bf16 v[220:223], v[96:99], v[236:239], v[220:223]\n"
    " s_waitcnt vmcnt(6)\n"
    " v_bitop3_b32 v2, v240, v243, s88 bitop3:0x7e\n"
    " v_mfma_f32_16x16x32_bf16 v[224:227], v[100:103], v[236:239], v[224:227]\n"
    " v_bitop3_b32 v3, v241, v242, s89 bitop3:0x7e\n"
    " v_mfma_f32_16x16x32_bf16 v[228:231], v[104:107], v[236:239], v[228:231]\n"
    " v_bitop3_b32 v2, v2, v3, s90 bitop3:0xa8\n"
    " v_cmp_ne_u32 vcc, 0, v2\n"
    " s_cbranch_vccnz .Lgs_145\n"
    ".Lgsb_146:\n"
    " buffer_load_dwordx4 v[64:67], v1, s[12:15], s76 offen offset:2048 sc1\n"
    " v_mfma_f32_16x16x32_bf16 v[216:219], v[108:111], v[240:243], v[216:219]\n"
    " ds_write_b128 v48, v[200:203] offset:0\n"
    " v_mfma_f32_16x16x32_bf16 v[220:223], v[112:115], v[240:243], v[220:223]\n"
    " s_waitcnt vmcnt(6)\n"
    " v_bitop3_b32 v2, v244, v247, s88 bitop3:0x7e\n"
    " v_mfma_f32_16x16x32_bf16 v[224:227], v[116:119], v[240:243], v[224:227]\n"
    " v_bitop3_b32 v3, v245, v246, s89 bitop3:0x7e\n"
    " v_mfma_f32_16x16x32_bf16 v[228:231], v[124:127], v[240:243], v[228:231]\n"
    " v_bitop3_b32 v2, v2, v3, s90 bitop3:0xa8\n"
    " v_cmp_ne_u32 vcc, 0, v2\n"
    " s_cbranch_vccnz .Lgs_148\n"
    ".Lgsb_149:\n"
    " buffer_load_dwordx4 v[68:71], v1, s[12:15], s76 offen offset:3072 sc1\n"
    " v_mfma_f32_16x16x32_bf16 v[216:219], v[120:123], v[244:247], v[216:219]\n"
    " ds_write_b128 v49, v[204:207] offset:1024\n"
    " v_mfma_f32_16x16x32_bf16 v[220:223], v[128:131], v[244:247], v[220:223]\n"
    " s_waitcnt vmcnt(6)\n"
    " v_bitop3_b32 v2, v248, v251, s88 bitop3:0x7e\n"
    " v_mfma_f32_16x16x32_bf16 v[224:227], v[132:135], v[244:247], v[224:227]\n"
    " v_bitop3_b32 v3, v249, v250, s89 bitop3:0x7e\n"
    " v_mfma_f32_16x16x32_bf16 v[228:231], v[136:139], v[244:247], v[228:231]\n"
    " v_bitop3_b32 v2, v2, v3, s90 bitop3:0xa8\n"
    " v_cmp_ne_u32 vcc, 0, v2\n"
    " s_cbranch_vccnz .Lgs_151\n"
    ".Lgsb_152:\n"
    " buffer_load_dwordx4 v[72:75], v1, s[12:15], s77 offen offset:0 sc1\n"
    " v_mfma_f32_16x16x32_bf16 v[216:219], v[140:143], v[248:251], v[216:219]\n"
    " ds_write_b128 v50, v[208:211] offset:2048\n"
    " v_mfma_f32_16x16x32_bf16 v[220:223], v[8:11], v[248:251], v[220:223]\n"
    " s_waitcnt vmcnt(6)\n"
    " v_bitop3_b32 v2, v252, v255, s88 bitop3:0x7e\n"
    " v_mfma_f32_16x16x32_bf16 v[224:227], v[12:15], v[248:251], v[224:227]\n"
    " v_bitop3_b32 v3, v253, v254, s89 bitop3:0x7e\n"
    " v_mfma_f32_16x16x32_bf16 v[228:231], v[16:19], v[248:251], v[228:231]\n"
    " v_bitop3_b32 v2, v2, v3, s90 bitop3:0xa8\n"
    " v_cmp_ne_u32 vcc, 0, v2\n"
    " s_cbranch_vccnz .Lgs_154\n"
    ".Lgsb_155:\n"
    " buffer_load_dwordx4 v[24:27], v1, s[12:15], s77 offen offset:1024 sc1\n"
    " v_mfma_f32_16x16x32_bf16 v[216:219], v[20:23], v[252:255], v[216:219]\n"
    " ds_write_b128 v51, v[212:215] offset:3072\n"
    " v_mfma_f32_16x16x32_bf16 v[220:223], v[144:147], v[252:255], v[220:223]\n"
    " s_waitcnt vmcnt(6)\n"
    " v_bitop3_b32 v2, v52, v55, s88 bitop3:0x7e\n"
    " v_mfma_f32_16x16x32_bf16 v[224:227], v[148:151], v[252:255], v[224:227]\n"
    " v_bitop3_b32 v3, v53, v54, s89 bitop3:0x7e\n"
    " v_mfma_f32_16x16x32_bf16 v[228:231], v[152:155], v[252:255], v[228:231]\n"
    " v_bitop3_b32 v2, v2, v3, s90 bitop3:0xa8\n"
    " v_cmp_ne_u32 vcc, 0, v2\n"
    " s_cbranch_vccnz .Lgs_157\n"
    ".Lgsb_158:\n"
    " buffer_load_dwordx4 v[28:31], v1, s[12:15], s77 offen offset:2048 sc1\n"
    " v_mfma_f32_16x16x32_bf16 v[216:219], v[156:159], v[52:55], v[216:219]\n"
    " v_mfma_f32_16x16x32_bf16 v[220:223], v[160:163], v[52:55], v[220:223]\n"
    " s_waitcnt vmcnt(6)\n"
    " v_bitop3_b32 v2, v56, v59, s88 bitop3:0x7e\n"
    " v_mfma_f32_16x16x32_bf16 v[224:227], v[164:167], v[52:55], v[224:227]\n"
    " v_bitop3_b32 v3, v57, v58, s89 bitop3:0x7e\n"
    " v_mfma_f32_16x16x32_bf16 v[228:231], v[168:171], v[52:55], v[228:231]\n"
    " v_bitop3_b32 v2, v2, v3, s90 bitop3:0xa8\n"
    " v_cmp_ne_u32 vcc, 0, v2\n"
    " s_cbranch_vccnz .Lgs_160\n"
    ".Lgsb_161:\n"
    " buffer_load_dwordx4 v[32:35], v1, s[12:15], s78 offen offset:0 sc1\n"
    " v_mfma_f32_16x16x32_bf16 v[200:203], v[76:79], v[56:59], 0\n"
    " v_mfma_f32_16x16x32_bf16 v[204:207], v[80:83], v[56:59], 0\n"
    " s_waitcnt vmcnt(6)\n"
    " v_bitop3_b32 v2, v60, v63, s88 bitop3:0x7e\n"
    " v_mfma_f32_16x16x32_bf16 v[208:211], v[84:87], v[56:59], 0\n"
    " v_bitop3_b32 v3, v61, v62, s89 bitop3:0x7e\n"
    " v_mfma_f32_16x16x32_bf16 v[212:215], v[88:91], v[56:59], 0\n"
    " v_bitop3_b32 v2, v2, v3, s90 bitop3:0xa8\n"
    " v_cmp_ne_u32 vcc, 0, v2\n"
    " s_cbranch_vccnz .Lgs_163\n"
    ".Lgsb_164:\n"
    " buffer_load_dwordx4 v[36:39], v1, s[12:15], s78 offen offset:1024 sc1\n"
    " v_mfma_f32_16x16x32_bf16 v[200:203], v[92:95], v[60:63], v[200:203]\n"
    " v_mfma_f32_16x16x32_bf16 v[204:207], v[96:99], v[60:63], v[204:207]\n"
    " s_waitcnt vmcnt(6)\n"
    " v_bitop3_b32 v2, v64, v67, s88 bitop3:0x7e\n"
    " v_mfma_f32_16x16x32_bf16 v[208:211], v[100:103], v[60:63], v[208:211]\n"
    " v_bitop3_b32 v3, v65, v66, s89 bitop3:0x7e\n"
    " v_mfma_f32_16x16x32_bf16 v[212:215], v[104:107], v[60:63], v[212:215]\n"
    " v_bitop3_b32 v2, v2, v3, s90 bitop3:0xa8\n"
    " v_cmp_ne_u32 vcc, 0, v2\n"
    " s_cbranch_vccnz .Lgs_166\n"
    ".Lgsb_167:\n"
    " buffer_load_dwordx4 v[40:43], v1, s[12:15], s78 offen offset:2048 sc1\n"
    " v_mfma_f32_16x16x32_bf16 v[200:203], v[108:111], v[64:67], v[200:203]\n"
    " ds_write_b128 v48, v[216:219] offset:4096\n"
    " v_mfma_f32_16x16x32_bf16 v[204:207], v[112:115], v[64:67], v[204:207]\n"
    " s_waitcnt vmcnt(6)\n"
    " v_bitop3_b32 v2, v68, v71, s88 bitop3:0x7e\n"
    " v_mfma_f32_16x16x32_bf16 v[208:211], v[116:119], v[64:67], v[208:211]\n"
    " v_bitop3_b32 v3, v69, v70, s89 bitop3:0x7e\n"
    " v_mfma_f32_16x16x32_bf16 v[212:215], v[124:127], v[64:67], v[212:215]\n"
    " v_bitop3_b32 v2, v2, v3, s90 bitop3:0xa8\n"
    " v_cmp_ne_u32 vcc, 0, v2\n"
    " s_cbranch_vccnz .Lgs_169\n"
    ".Lgsb_170:\n"
    " buffer_load_dwordx4 v[44:47], v1, s[12:15], s78 offen offset:3072 sc1\n"
    " v_mfma_f32_16x16x32_bf16 v[200:203], v[120:123], v[68:71], v[200:203]\n"
    " ds_write_b128 v49, v[220:223] offset:5120\n"
    " v_mfma_f32_16x16x32_bf16 v[204:207], v[128:131], v[68:71], v[204:207]\n"
    " s_waitcnt vmcnt(6)\n"
    " v_bitop3_b32 v2, v72, v75, s88 bitop3:0x7e\n"
    " v_mfma_f32_16x16x32_bf16 v[208:211], v[132:135], v[68:71], v[208:211]\n"
    " v_bitop3_b32 v3, v73, v74, s89 bitop3:0x7e\n"
    " v_mfma_f32_16x16x32_bf16 v[212:215], v[136:139], v[68:71], v[212:215]\n"
    " v_bitop3_b32 v2, v2, v3, s90 bitop3:0xa8\n"
    " v_cmp_ne_u32 vcc, 0, v2\n"
    " s_cbranch_vccnz .Lgs_172\n"
    ".Lgsb_173:\n"
    " buffer_load_dwordx4 v[172:175], v1, s[12:15], s79 offen offset:0 sc1\n"
    " v_mfma_f32_16x16x32_bf16 v[200:203], v[140:143], v[72:75], v[200:203]\n"
    " ds_write_b128 v50, v[224:227] offset:6144\n"
    " v_mfma_f32_16x16x32_bf16 v[204:207], v[8:11], v[72:75], v[204:207]\n"
    " s_waitcnt vmcnt(6)\n"
    " v_bitop3_b32 v2, v24, v27, s88 bitop3:0x7e\n"
    " v_mfma_f32_16x16x32_bf16 v[208:211], v[12:15], v[72:75], v[208:211]\n"
    " v_bitop3_b32 v3, v25, v26, s89 bitop3:0x7e\n"
    " v_mfma_f32_16x16x32_bf16 v[212:215], v[16:19], v[72:75], v[212:215]\n"
    " v_bitop3_b32 v2, v2, v3, s90 bitop3:0xa8\n"
    " v_cmp_ne_u32 vcc, 0, v2\n"
    " s_cbranch_vccnz .Lgs_175\n"
    ".Lgsb_176:\n"
    " buffer_load_dwordx4 v[176:179], v1, s[12:15], s79 offen offset:1024 sc1\n"
    " v_mfma_f32_16x16x32_bf16 v[200:203], v[20:23], v[24:27], v[200:203]\n"
    " ds_write_b128 v51, v[228:231] offset:7168\n"
    " v_mfma_f32_16x16x32_bf16 v[204:207], v[144:147], v[24:27], v[204:207]\n"
    " s_waitcnt vmcnt(6)\n"
    " v_bitop3_b32 v2, v28, v31, s88 bitop3:0x7e\n"
    " v_mfma_f32_16x16x32_bf16 v[208:211], v[148:151], v[24:27], v[208:211]\n"
    " v_bitop3_b32 v3, v29, v30, s89 bitop3:0x7e\n"
    " v_mfma_f32_16x16x32_bf16 v[212:215], v[152:155], v[24:27], v[212:215]\n"
    " v_bitop3_b32 v2, v2, v3, s90 bitop3:0xa8\n"
    " v_cmp_ne_u32 vcc, 0, v2\n"
    " s_cbranch_vccnz .Lgs_178\n"
    ".Lgsb_179:\n"
    " buffer_load_dwordx4 v[180:183], v1, s[12:15], s79 offen offset:2048 sc1\n"
    " v_mfma_f32_16x16x32_bf16 v[200:203], v[156:159], v[28:31], v[200:203]\n"
    " v_mfma_f32_16x16x32_bf16 v[204:207], v[160:163], v[28:31], v[204:207]\n"
    " s_waitcnt vmcnt(6)\n"
    " v_bitop3_b32 v2, v32, v35, s88 bitop3:0x7e\n"
    " v_mfma_f32_16x16x32_bf16 v[208:211], v[164:167], v[28:31], v[208:211]\n"
    " v_bitop3_b32 v3, v33, v34, s89 bitop3:0x7e\n"
    " v_mfma_f32_16x16x32_bf16 v[212:215], v[168:171], v[28:31], v[212:215]\n"
    " v_bitop3_b32 v2, v2, v3, s90 bitop3:0xa8\n"
    " v_cmp_ne_u32 vcc, 0, v2\n"
    " s_cbranch_vccnz .Lgs_181\n"
    ".Lgsb_182:\n"
    " v_mfma_f32_16x16x32_bf16 v[216:219], v[76:79], v[32:35], 0\n"
    " v_mfma_f32_16x16x32_bf16 v[220:223], v[80:83], v[32:35], 0\n"
    " s_waitcnt vmcnt(5)\n"
    " v_bitop3_b32 v2, v36, v39, s88 bitop3:0x7e\n"
    " v_mfma_f32_16x16x32_bf16 v[224:227], v[84:87], v[32:35], 0\n"
    " v_bitop3_b32 v3, v37, v38, s89 bitop3:0x7e\n"
    " v_mfma_f32_16x16x32_bf16 v[228:231], v[88:91], v[32:35], 0\n"
    " v_bitop3_b32 v2, v2, v3, s90 bitop3:0xa8\n"
    " v_cmp_ne_u32 vcc, 0, v2\n"
    " s_cbranch_vccnz .Lgs_184\n"
    ".Lgsb_185:\n"
    " v_mfma_f32_16x16x32_bf16 v[216:219], v[92:95], v[36:39], v[216:219]\n"
    " v_mfma_f32_16x16x32_bf16 v[220:223], v[96:99], v[36:39], v[220:223]\n"
    " s_waitcnt vmcnt(4)\n"
    " v_bitop3_b32 v2, v40, v43, s88 bitop3:0x7e\n"
    " v_mfma_f32_16x16x32_bf16 v[224:227], v[100:103], v[36:39], v[224:227]\n"
    " v_bitop3_b32 v3, v41, v42, s89 bitop3:0x7e\n"
    " v_mfma_f32_16x16x32_bf16 v[228:231], v[104:107], v[36:39], v[228:231]\n"
    " v_bitop3_b32 v2, v2, v3, s90 bitop3:0xa8\n"
    " v_cmp_ne_u32 vcc, 0, v2\n"
    " s_cbranch_vccnz .Lgs_187\n"
    ".Lgsb_188:\n"
    " v_mfma_f32_16x16x32_bf16 v[216:219], v[108:111], v[40:43], v[216:219]\n"
    " v_subrev_u32 v48, 66560, v48\n"
    " v_subrev_u32 v49, 66560, v49\n"
    " v_subrev_u32 v50, 66560, v50\n"
    " v_subrev_u32 v51, 66560, v51\n"
    " v_mfma_f32_16x16x32_bf16 v[220:223], v[112:115], v[40:43], v[220:223]\n"
    " s_waitcnt vmcnt(3)\n"
    " v_bitop3_b32 v2, v44, v47, s88 bitop3:0x7e\n"
    " v_mfma_f32_16x16x32_bf16 v[224:227], v[116:119], v[40:43], v[224:227]\n"
    " v_bitop3_b32 v3, v45, v46, s89 bitop3:0x7e\n"
    " v_mfma_f32_16x16x32_bf16 v[228:231], v[124:127], v[40:43], v[228:231]\n"
    " v_bitop3_b32 v2, v2, v3, s90 bitop3:0xa8\n"
    " v_cmp_ne_u32 vcc, 0, v2\n"
    " s_cbranch_vccnz .Lgs_190\n"
    ".Lgsb_191:\n"
    " v_mfma_f32_16x16x32_bf16 v[216:219], v[120:123], v[44:47], v[216:219]\n"
    " ds_write_b128 v48, v[200:203] offset:0\n"
    " v_mfma_f32_16x16x32_bf16 v[220:223], v[128:131], v[44:47], v[220:223]\n"
    " s_waitcnt vmcnt(2)\n"
    " v_bitop3_b32 v2, v172, v175, s88 bitop3:0x7e\n"
    " v_mfma_f32_16x16x32_bf16 v[224:227], v[132:135], v[44:47], v[224:227]\n"
    " v_bitop3_b32 v3, v173, v174, s89 bitop3:0x7e\n"
    " v_mfma_f32_16x16x32_bf16 v[228:231], v[136:139], v[44:47], v[228:231]\n"
    " v_bitop3_b32 v2, v2, v3, s90 bitop3:0xa8\n"
    " v_cmp_ne_u32 vcc, 0, v2\n"
    " s_cbranch_vccnz .Lgs_193\n"
    ".Lgsb_194:\n"
    " v_mfma_f32_16x16x32_bf16 v[216:219], v[140:143], v[172:175], v[216:219]\n"
    " ds_write_b128 v49, v[204:207] offset:1024\n"
    " v_mfma_f32_16x16x32_bf16 v[220:223], v[8:11], v[172:175], v[220:223]\n"
    " s_waitcnt vmcnt(1)\n"
    " v_bitop3_b32 v2, v176, v179, s88 bitop3:0x7e\n"
    " v_mfma_f32_16x16x32_bf16 v[224:227], v[12:15], v[172:175], v[224:227]\n"
    " v_bitop3_b32 v3, v177, v178, s89 bitop3:0x7e\n"
    " v_mfma_f32_16x16x32_bf16 v[228:231], v[16:19], v[172:175], v[228:231]\n"
    " v_bitop3_b32 v2, v2, v3, s90 bitop3:0xa8\n"
    " v_cmp_ne_u32 vcc, 0, v2\n"
    " s_cbranch_vccnz .Lgs_196\n"
    ".Lgsb_197:\n"
    " v_mfma_f32_16x16x32_bf16 v[216:219], v[20:23], v[176:179], v[216:219]\n"
    " ds_write_b128 v50, v[208:211] offset:2048\n"
    " v_mfma_f32_16x16x32_bf16 v[220:223], v[144:147], v[176:179], v[220:223]\n"
    " s_waitcnt vmcnt(0)\n"
    " v_bitop3_b32 v2, v180, v183, s88 bitop3:0x7e\n"
    " v_mfma_f32_16x16x32_bf16 v[224:227], v[148:151], v[176:179], v[224:227]\n"
    " v_bitop3_b32 v3, v181, v182, s89 bitop3:0x7e\n"
    " v_mfma_f32_16x16x32_bf16 v[228:231], v[152:155], v[176:179], v[228:231]\n"
    " v_bitop3_b32 v2, v2, v3, s90 bitop3:0xa8\n"
    " v_cmp_ne_u32 vcc, 0, v2\n"
    " s_cbranch_vccnz .Lgs_199\n"
    ".Lgsb_200:\n"
    " v_mfma_f32_16x16x32_bf16 v[216:219], v[156:159], v[180:183], v[216:219]\n"
    " ds_write_b128 v51, v[212:215] offset:3072\n"
    " v_mfma_f32_16x16x32_bf16 v[220:223], v[160:163], v[180:183], v[220:223]\n"
    " v_mfma_f32_16x16x32_bf16 v[224:227], v[164:167], v[180:183], v[224:227]\n"
    " v_mfma_f32_16x16x32_bf16 v[228:231], v[168:171], v[180:183], v[228:231]\n"
    " v_and_b32 v5, 7, v0\n"
    " v_bfe_u32 v6, v0, 3, 4\n"
    " v_lshlrev_b32 v7, 1, v5\n"
    " v_xor_b32 v6, v6, v7\n"
    " v_lshlrev_b32 v6, 4, v6\n"
    " v_lshl_add_u32 v6, v5, 9, v6\n"
    " v_bfe_u32 v7, v0, 7, 1\n"
    " v_lshl_add_u32 v6, v7, 12, v6\n"
    " ds_write_b128 v48, v[216:219] offset:4096\n"
    " ds_write_b128 v49, v[220:223] offset:5120\n"
    " ds_write_b128 v50, v[224:227] offset:6144\n"
    " ds_write_b128 v51, v[228:231] offset:7168\n"
    " s_cmp_lt_u32 s44, 4\n"
    " s_cselect_b32 s62, 66560, 0\n"
    " v_add_u32 v6, s62, v6\n"
    " s_lshl_b32 s60, s48, 16\n"
    " s_lshl_b32 s61, s49, 7\n"
    " s_add_u32 s60, s60, s61\n"
    " v_lshrrev_b32 v7, 3, v0\n"
    " v_lshlrev_b32 v5, 4, v5\n"
    " v_lshl_add_u32 v5, v7, 10, v5\n"
    " v_add_u32 v5, s60, v5\n"
    " s_waitcnt lgkmcnt(0)\n"
    ".Lb7:\n"
    " s_barrier\n"
    " ds_read_b128 v[128:131], v6 offset:0\n"
    " ds_read_b128 v[132:135], v6 offset:256\n"
    " ds_read_b128 v[136:139], v6 offset:8192\n"
    " ds_read_b128 v[140:143], v6 offset:8448\n"
    " ds_read_b128 v[144:147], v6 offset:16384\n"
    " ds_read_b128 v[148:151], v6 offset:16640\n"
    " ds_read_b128 v[152:155], v6 offset:24576\n"
    " ds_read_b128 v[156:159], v6 offset:24832\n"
    " ds_read_b128 v[160:163], v6 offset:32768\n"
    " ds_read_b128 v[164:167], v6 offset:33024\n"
    " ds_read_b128 v[168:171], v6 offset:40960\n"
    " ds_read_b128 v[172:175], v6 offset:41216\n"
    " ds_read_b128 v[176:179], v6 offset:49152\n"
    " ds_read_b128 v[180:183], v6 offset:49408\n"
    " ds_read_b128 v[184:187], v6 offset:57344\n"
    " ds_read_b128 v[188:191], v6 offset:57600\n"
    " s_waitcnt lgkmcnt(13)\n"
    " v_pk_add_f32 v[128:129], v[128:129], v[136:137]\n"
    " v_pk_add_f32 v[130:131], v[130:131], v[138:139]\n"
    " s_waitcnt lgkmcnt(12)\n"
    " v_pk_add_f32 v[132:133], v[132:133], v[140:141]\n"
    " v_pk_add_f32 v[134:135], v[134:135], v[142:143]\n"
    " s_waitcnt lgkmcnt(11)\n"
    " v_pk_add_f32 v[128:129], v[128:129], v[144:145]\n"
    " v_pk_add_f32 v[130:131], v[130:131], v[146:147]\n"
    " s_waitcnt lgkmcnt(10)\n"
    " v_pk_add_f32 v[132:133], v[132:133], v[148:149]\n"
    " v_pk_add_f32 v[134:135], v[134:135], v[150:151]\n"
    " s_waitcnt lgkmcnt(9)\n"
    " v_pk_add_f32 v[128:129], v[128:129], v[152:153]\n"
    " v_pk_add_f32 v[130:131], v[130:131], v[154:155]\n"
    " s_waitcnt lgkmcnt(8)\n"
    " v_pk_add_f32 v[132:133], v[132:133], v[156:157]\n"
    " v_pk_add_f32 v[134:135], v[134:135], v[158:159]\n"
    " s_waitcnt lgkmcnt(7)\n"
    " v_pk_add_f32 v[128:129], v[128:129], v[160:161]\n"
    " v_pk_add_f32 v[130:131], v[130:131], v[162:163]\n"
    " s_waitcnt lgkmcnt(6)\n"
    " v_pk_add_f32 v[132:133], v[132:133], v[164:165]\n"
    " v_pk_add_f32 v[134:135], v[134:135], v[166:167]\n"
    " s_waitcnt lgkmcnt(5)\n"
    " v_pk_add_f32 v[128:129], v[128:129], v[168:169]\n"
    " v_pk_add_f32 v[130:131], v[130:131], v[170:171]\n"
    " s_waitcnt lgkmcnt(4)\n"
    " v_pk_add_f32 v[132:133], v[132:133], v[172:173]\n"
    " v_pk_add_f32 v[134:135], v[134:135], v[174:175]\n"
    " s_waitcnt lgkmcnt(3)\n"
    " v_pk_add_f32 v[128:129], v[128:129], v[176:177]\n"
    " v_pk_add_f32 v[130:131], v[130:131], v[178:179]\n"
    " s_waitcnt lgkmcnt(2)\n"
    " v_pk_add_f32 v[132:133], v[132:133], v[180:181]\n"
    " v_pk_add_f32 v[134:135], v[134:135], v[182:183]\n"
    " s_waitcnt lgkmcnt(1)\n"
    " v_pk_add_f32 v[128:129], v[128:129], v[184:185]\n"
    " v_pk_add_f32 v[130:131], v[130:131], v[186:187]\n"
    " s_waitcnt lgkmcnt(0)\n"
    " v_pk_add_f32 v[132:133], v[132:133], v[188:189]\n"
    " v_pk_add_f32 v[134:135], v[134:135], v[190:191]\n"
    " v_cvt_pk_bf16_f32 v8, v128, v129\n"
    " v_cvt_pk_bf16_f32 v9, v130, v131\n"
    " v_cvt_pk_bf16_f32 v10, v132, v133\n"
    " v_cvt_pk_bf16_f32 v11, v134, v135\n"
    " buffer_store_dwordx4 v[8:11], v5, s[36:39], 0 offen\n"
    " s_endpgm\n"
    ".Lmid7:\n"
    " s_lshr_b32 s60, s49, 1\n"
    " s_lshl_b32 s60, s60, 7\n"
    " s_add_u32 s60, s60, 167936\n"
    " v_and_b32 v6, 63, v0\n"
    " v_min_u32 v6, 55, v6\n"
    " v_lshl_add_u32 v6, v6, 9, s60\n"
    " buffer_load_dwordx2 v[2:3], v6, s[12:15], 0 offen sc1\n"
    " v_mov_b32 v76, 0x3f803f80\n"
    " v_mov_b32 v77, 0x3f803f80\n"
    " v_mov_b32 v78, 0x3f803f80\n"
    " v_mov_b32 v79, 0x3f803f80\n"
    " s_waitcnt vmcnt(8)\n"
    " ds_write_b128 v7, v[8:11] offset:0\n"
    " v_max3_u32 v5, v8, v9, v10\n"
    " v_max3_u32 v5, v10, v11, v5\n"
    " s_waitcnt vmcnt(7)\n"
    " ds_write_b128 v7, v[12:15] offset:1024\n"
    " v_max3_u32 v5, v12, v13, v5\n"
    " v_max3_u32 v5, v14, v15, v5\n"
    " s_waitcnt vmcnt(6)\n"
    " ds_write_b128 v7, v[16:19] offset:2048\n"
    " v_max3_u32 v5, v16, v17, v5\n"
    " v_max3_u32 v5, v18, v19, v5\n"
    " s_waitcnt vmcnt(5)\n"
    " ds_write_b128 v7, v[20:23] offset:3072\n"
    " v_max3_u32 v5, v20, v21, v5\n"
    " v_max3_u32 v5, v22, v23, v5\n"
    " s_waitcnt vmcnt(4)\n"
    " ds_write_b128 v7, v[24:27] offset:4096\n"
    " v_max3_u32 v5, v24, v25, v5\n"
    " v_max3_u32 v5, v26, v27, v5\n"
    " s_waitcnt vmcnt(3)\n"
    " ds_write_b128 v7, v[28:31] offset:5120\n"
    " v_max3_u32 v5, v28, v29, v5\n"
    " v_max3_u32 v5, v30, v31, v5\n"
    " s_waitcnt vmcnt(2)\n"
    " ds_write_b128 v7, v[32:35] offset:6144\n"
    " v_max3_u32 v5, v32, v33, v5\n"
    " v_max3_u32 v5, v34, v35, v5\n"
    " s_waitcnt vmcnt(1)\n"
    " ds_write_b128 v7, v[36:39] offset:7168\n"
    " v_max3_u32 v5, v36, v37, v5\n"
    " v_max3_u32 v5, v38, v39, v5\n"
    " v_cmp_eq_u32 vcc, -1, v5\n"
    " s_cbranch_vccnz .Lhs_24\n"
    ".Lhsb_25:\n"
    " s_waitcnt vmcnt(0)\n"
    " v_xor_b32 v2, s50, v2\n"
    " v_bitop3_b32 v2, v3, s51, v2 bitop3:0xbe\n"
    " v_cmp_ne_u32 vcc, 0, v2\n"
    " s_cbranch_vccnz .Lfl_27\n"
    ".Lflb_28:\n"
    " v_and_b32 v7, 15, v0\n"
    " v_lshlrev_b32 v7, 3, v7\n"
    " v_add_u32 v7, 65536, v7\n"
    " s_waitcnt lgkmcnt(0)\n"
    " s_barrier\n"
    " s_cmp_lg_u32 s2, 0\n"
    " s_cbranch_scc1 .Lnoep\n"
    " v_mov_b32 v5, s50\n"
    " s_mov_b64 exec, 1\n"
    " buffer_store_dword v5, off, s[12:15], 0 sc1\n"
    " s_mov_b64 exec, -1\n"
    ".Lnoep:\n"
    " v_and_b32 v6, 1, v0\n"
    " v_cmp_eq_u32 s[80:81], 1, v6\n"
    " v_and_b32 v6, 2, v0\n"
    " v_cmp_eq_u32 s[82:83], 2, v6\n"
    " v_bfe_u32 v6, v0, 2, 2\n"
    " v_bfe_u32 v5, v0, 4, 2\n"
    " v_cmp_eq_u32 s[84:85], v6, v5\n"
    " s_mov_b32 s65, 0x3b000000\n"
    " ds_read_b64_tr_b16 v[52:53], v48 offset:0\n"
    " ds_read_b64_tr_b16 v[54:55], v48 offset:512\n"
    " ds_read_b64_tr_b16 v[56:57], v49 offset:0\n"
    " ds_read_b64_tr_b16 v[58:59], v49 offset:512\n"
    " ds_read_b64_tr_b16 v[60:61], v48 offset:4096\n"
    " ds_read_b64_tr_b16 v[62:63], v48 offset:4608\n"
    " ds_read_b64_tr_b16 v[64:65], v49 offset:4096\n"
    " ds_read_b64_tr_b16 v[66:67], v49 offset:4608\n"
    " buffer_load_dwordx4 v[80:83], v1, s[12:15], s68 offen offset:0 sc1\n"
    " ds_read_b64_tr_b16 v[68:69], v48 offset:8192\n"
    " ds_read_b64_tr_b16 v[70:71], v48 offset:8704\n"
    " s_waitcnt lgkmcnt(8)\n"
    " v_mfma_f32_16x16x32_bf16 v[8:11], v[76:79], v[52:55], 0\n"
    " v_mfma_f32_16x16x32_bf16 v[16:19], v[52:55], v[52:55], 0\n"
    " ds_read_b64_tr_b16 v[72:73], v49 offset:8192\n"
    " ds_read_b64_tr_b16 v[74:75], v49 offset:8704\n"
    " s_waitcnt lgkmcnt(8)\n"
    " v_mfma_f32_16x16x32_bf16 v[12:15], v[76:79], v[56:59], 0\n"
    " v_mfma_f32_16x16x32_bf16 v[20:23], v[56:59], v[56:59], 0\n"
    " buffer_load_dwordx4 v[84:87], v1, s[12:15], s68 offen offset:1024 sc1\n"
    " ds_read_b64_tr_b16 v[52:53], v48 offset:12288\n"
    " ds_read_b64_tr_b16 v[54:55], v48 offset:12800\n"
    " s_waitcnt lgkmcnt(8)\n"
    " v_mfma_f32_16x16x32_bf16 v[8:11], v[76:79], v[60:63], v[8:11]\n"
    " v_mfma_f32_16x16x32_bf16 v[16:19], v[60:63], v[60:63], v[16:19]\n"
    " ds_read_b64_tr_b16 v[56:57], v49 offset:12288\n"
    " ds_read_b64_tr_b16 v[58:59], v49 offset:12800\n"
    " s_waitcnt lgkmcnt(8)\n"
    " v_mfma_f32_16x16x32_bf16 v[12:15], v[76:79], v[64:67], v[12:15]\n"
    " v_mfma_f32_16x16x32_bf16 v[20:23], v[64:67], v[64:67], v[20:23]\n"
    " buffer_load_dwordx4 v[88:91], v1, s[12:15], s68 offen offset:2048 sc1\n"
    " ds_read_b64_tr_b16 v[60:61], v48 offset:16384\n"
    " ds_read_b64_tr_b16 v[62:63], v48 offset:16896\n"
    " s_waitcnt lgkmcnt(8)\n"
    " v_mfma_f32_16x16x32_bf16 v[8:11], v[76:79], v[68:71], v[8:11]\n"
    " v_mfma_f32_16x16x32_bf16 v[16:19], v[68:71], v[68:71], v[16:19]\n"
    " ds_read_b64_tr_b16 v[64:65], v49 offset:16384\n"
    " ds_read_b64_tr_b16 v[66:67], v49 offset:16896\n"
    " s_waitcnt lgkmcnt(8)\n"
    " v_mfma_f32_16x16x32_bf16 v[12:15], v[76:79], v[72:75], v[12:15]\n"
    " v_mfma_f32_16x16x32_bf16 v[20:23], v[72:75], v[72:75], v[20:23]\n"
    " buffer_load_dwordx4 v[92:95], v1, s[12:15], s68 offen offset:3072 sc1\n"
    " ds_read_b64_tr_b16 v[68:69], v48 offset:20480\n"
    " ds_read_b64_tr_b16 v[70:71], v48 offset:20992\n"
    " s_waitcnt lgkmcnt(8)\n"
    " v_mfma_f32_16x16x32_bf16 v[8:11], v[76:79], v[52:55], v[8:11]\n"
    " v_mfma_f32_16x16x32_bf16 v[16:19], v[52:55], v[52:55], v[16:19]\n"
    " ds_read_b64_tr_b16 v[72:73], v49 offset:20480\n"
    " ds_read_b64_tr_b16 v[74:75], v49 offset:20992\n"
    " s_waitcnt lgkmcnt(8)\n"
    " v_mfma_f32_16x16x32_bf16 v[12:15], v[76:79], v[56:59], v[12:15]\n"
    " v_mfma_f32_16x16x32_bf16 v[20:23], v[56:59], v[56:59], v[20:23]\n"
    " s_add_u32 s68, s68, 4096\n"
    " buffer_load_dwordx4 v[96:99], v1, s[12:15], s68 offen offset:0 sc1\n"
    " ds_read_b64_tr_b16 v[52:53], v48 offset:24576\n"
    " ds_read_b64_tr_b16 v[54:55], v48 offset:25088\n"
    " s_waitcnt lgkmcnt(8)\n"
    " v_mfma_f32_16x16x32_bf16 v[8:11], v[76:79], v[60:63], v[8:11]\n"
    " v_mfma_f32_16x16x32_bf16 v[16:19], v[60:63], v[60:63], v[16:19]\n"
    " ds_read_b64_tr_b16 v[56:57], v49 offset:24576\n"
    " ds_read_b64_tr_b16 v[58:59], v49 offset:25088\n"
    " s_waitcnt lgkmcnt(8)\n"
    " v_mfma_f32_16x16x32_bf16 v[12:15], v[76:79], v[64:67], v[12:15]\n"
    " v_mfma_f32_16x16x32_bf16 v[20:23], v[64:67], v[64:67], v[20:23]\n"
    " buffer_load_dwordx4 v[100:103], v1, s[12:15], s68 offen offset:1024 sc1\n"
    " ds_read_b64_tr_b16 v[60:61], v48 offset:28672\n"
    " ds_read_b64_tr_b16 v[62:63], v48 offset:29184\n"
    " s_waitcnt lgkmcnt(8)\n"
    " v_mfma_f32_16x16x32_bf16 v[8:11], v[76:79], v[68:71], v[8:11]\n"
    " v_mfma_f32_16x16x32_bf16 v[16:19], v[68:71], v[68:71], v[16:19]\n"
    " ds_read_b64_tr_b16 v[64:65], v49 offset:28672\n"
    " ds_read_b64_tr_b16 v[66:67], v49 offset:29184\n"
    " s_waitcnt lgkmcnt(8)\n"
    " v_mfma_f32_16x16x32_bf16 v[12:15], v[76:79], v[72:75], v[12:15]\n"
    " v_mfma_f32_16x16x32_bf16 v[20:23], v[72:75], v[72:75], v[20:23]\n"
    " buffer_load_dwordx4 v[104:107], v1, s[12:15], s68 offen offset:2048 sc1\n"
    " ds_read_b64_tr_b16 v[68:69], v48 offset:32768\n"
    " ds_read_b64_tr_b16 v[70:71], v48 offset:33280\n"
    " s_waitcnt lgkmcnt(8)\n"
    " v_mfma_f32_16x16x32_bf16 v[8:11], v[76:79], v[52:55], v[8:11]\n"
    " v_mfma_f32_16x16x32_bf16 v[16:19], v[52:55], v[52:55], v[16:19]\n"
    " ds_read_b64_tr_b16 v[72:73], v49 offset:32768\n"
    " ds_read_b64_tr_b16 v[74:75], v49 offset:33280\n"
    " s_waitcnt lgkmcnt(8)\n"
    " v_mfma_f32_16x16x32_bf16 v[12:15], v[76:79], v[56:59], v[12:15]\n"
    " v_mfma_f32_16x16x32_bf16 v[20:23], v[56:59], v[56:59], v[20:23]\n"
    " buffer_load_dwordx4 v[108:111], v1, s[12:15], s68 offen offset:3072 sc1\n"
    " ds_read_b64_tr_b16 v[52:53], v48 offset:36864\n"
    " ds_read_b64_tr_b16 v[54:55], v48 offset:37376\n"
    " s_waitcnt lgkmcnt(8)\n"
    " v_mfma_f32_16x16x32_bf16 v[8:11], v[76:79], v[60:63], v[8:11]\n"
    " v_mfma_f32_16x16x32_bf16 v[16:19], v[60:63], v[60:63], v[16:19]\n"
    " ds_read_b64_tr_b16 v[56:57], v49 offset:36864\n"
    " ds_read_b64_tr_b16 v[58:59], v49 offset:37376\n"
    " s_waitcnt lgkmcnt(8)\n"
    " v_mfma_f32_16x16x32_bf16 v[12:15], v[76:79], v[64:67], v[12:15]\n"
    " v_mfma_f32_16x16x32_bf16 v[20:23], v[64:67], v[64:67], v[20:23]\n"
    " s_add_u32 s68, s68, 4096\n"
    " buffer_load_dwordx4 v[112:115], v1, s[12:15], s68 offen offset:0 sc1\n"
    " ds_read_b64_tr_b16 v[60:61], v48 offset:40960\n"
    " ds_read_b64_tr_b16 v[62:63], v48 offset:41472\n"
    " s_waitcnt lgkmcnt(8)\n"
    " v_mfma_f32_16x16x32_bf16 v[8:11], v[76:79], v[68:71], v[8:11]\n"
    " v_mfma_f32_16x16x32_bf16 v[16:19], v[68:71], v[68:71], v[16:19]\n"
    " ds_read_b64_tr_b16 v[64:65], v49 offset:40960\n"
    " ds_read_b64_tr_b16 v[66:67], v49 offset:41472\n"
    " s_waitcnt lgkmcnt(8)\n"
    " v_mfma_f32_16x16x32_bf16 v[12:15], v[76:79], v[72:75], v[12:15]\n"
    " v_mfma_f32_16x16x32_bf16 v[20:23], v[72:75], v[72:75], v[20:23]\n"
    " buffer_load_dwordx4 v[116:119], v1, s[12:15], s68 offen offset:1024 sc1\n"
    " ds_read_b64_tr_b16 v[68:69], v48 offset:45056\n"
    " ds_read_b64_tr_b16 v[70:71], v48 offset:45568\n"
    " s_waitcnt lgkmcnt(8)\n"
    " v_mfma_f32_16x16x32_bf16 v[8:11], v[76:79], v[52:55], v[8:11]\n"
    " v_mfma_f32_16x16x32_bf16 v[16:19], v[52:55], v[52:55], v[16:19]\n"
    " ds_read_b64_tr_b16 v[72:73], v49 offset:45056\n"
    " ds_read_b64_tr_b16 v[74:75], v49 offset:45568\n"
    " s_waitcnt lgkmcnt(8)\n"
    " v_mfma_f32_16x16x32_bf16 v[12:15], v[76:79], v[56:59], v[12:15]\n"
    " v_mfma_f32_16x16x32_bf16 v[20:23], v[56:59], v[56:59], v[20:23]\n"
    " buffer_load_dwordx4 v[120:123], v1, s[12:15], s68 offen offset:2048 sc1\n"
    " ds_read_b64_tr_b16 v[52:53], v48 offset:49152\n"
    " ds_read_b64_tr_b16 v[54:55], v48 offset:49664\n"
    " s_waitcnt lgkmcnt(8)\n"
    " v_mfma_f32_16x16x32_bf16 v[8:11], v[76:79], v[60:63], v[8:11]\n"
    " v_mfma_f32_16x16x32_bf16 v[16:19], v[60:63], v[60:63], v[16:19]\n"
    " ds_read_b64_tr_b16 v[56:57], v49 offset:49152\n"
    " ds_read_b64_tr_b16 v[58:59], v49 offset:49664\n"
    " s_waitcnt lgkmcnt(8)\n"
    " v_mfma_f32_16x16x32_bf16 v[12:15], v[76:79], v[64:67], v[12:15]\n"
    " v_mfma_f32_16x16x32_bf16 v[20:23], v[64:67], v[64:67], v[20:23]\n"
    " buffer_load_dwordx4 v[124:127], v1, s[12:15], s68 offen offset:3072 sc1\n"
    " ds_read_b64_tr_b16 v[60:61], v48 offset:53248\n"
    " ds_read_b64_tr_b16 v[62:63], v48 offset:53760\n"
    " s_waitcnt lgkmcnt(8)\n"
    " v_mfma_f32_16x16x32_bf16 v[8:11], v[76:79], v[68:71], v[8:11]\n"
    " v_mfma_f32_16x16x32_bf16 v[16:19], v[68:71], v[68:71], v[16:19]\n"
    " ds_read_b64_tr_b16 v[64:65], v49 offset:53248\n"
    " ds_read_b64_tr_b16 v[66:67], v49 offset:53760\n"
    " s_waitcnt lgkmcnt(8)\n"
    " v_mfma_f32_16x16x32_bf16 v[12:15], v[76:79], v[72:75], v[12:15]\n"
    " v_mfma_f32_16x16x32_bf16 v[20:23], v[72:75], v[72:75], v[20:23]\n"
    " s_add_u32 s68, s68, 4096\n"
    " buffer_load_dwordx4 v[128:131], v1, s[12:15], s68 offen offset:0 sc1\n"
    " ds_read_b64_tr_b16 v[68:69], v48 offset:57344\n"
    " ds_read_b64_tr_b16 v[70:71], v48 offset:57856\n"
    " s_waitcnt lgkmcnt(8)\n"
    " v_mfma_f32_16x16x32_bf16 v[8:11], v[76:79], v[52:55], v[8:11]\n"
    " v_mfma_f32_16x16x32_bf16 v[16:19], v[52:55], v[52:55], v[16:19]\n"
    " ds_read_b64_tr_b16 v[72:73], v49 offset:57344\n"
    " ds_read_b64_tr_b16 v[74:75], v49 offset:57856\n"
    " s_waitcnt lgkmcnt(8)\n"
    " v_mfma_f32_16x16x32_bf16 v[12:15], v[76:79], v[56:59], v[12:15]\n"
    " v_mfma_f32_16x16x32_bf16 v[20:23], v[56:59], v[56:59], v[20:23]\n"
    " buffer_load_dwordx4 v[132:135], v1, s[12:15], s68 offen offset:1024 sc1\n"
    " ds_read_b64_tr_b16 v[52:53], v48 offset:61440\n"
    " ds_read_b64_tr_b16 v[54:55], v48 offset:61952\n"
    " s_waitcnt lgkmcnt(8)\n"
    " v_mfma_f32_16x16x32_bf16 v[8:11], v[76:79], v[60:63], v[8:11]\n"
    " v_mfma_f32_16x16x32_bf16 v[16:19], v[60:63], v[60:63], v[16:19]\n"
    " ds_read_b64_tr_b16 v[56:57], v49 offset:61440\n"
    " ds_read_b64_tr_b16 v[58:59], v49 offset:61952\n"
    " s_waitcnt lgkmcnt(8)\n"
    " v_mfma_f32_16x16x32_bf16 v[12:15], v[76:79], v[64:67], v[12:15]\n"
    " v_mfma_f32_16x16x32_bf16 v[20:23], v[64:67], v[64:67], v[20:23]\n"
    " buffer_load_dwordx4 v[136:139], v1, s[12:15], s68 offen offset:2048 sc1\n"
    " ds_read_b64_tr_b16 v[60:61], v50 offset:0\n"
    " ds_read_b64_tr_b16 v[62:63], v50 offset:512\n"
    " s_waitcnt lgkmcnt(8)\n"
    " v_mfma_f32_16x16x32_bf16 v[8:11], v[76:79], v[68:71], v[8:11]\n"
    " v_mfma_f32_16x16x32_bf16 v[16:19], v[68:71], v[68:71], v[16:19]\n"
    " ds_read_b64_tr_b16 v[64:65], v51 offset:0\n"
    " ds_read_b64_tr_b16 v[66:67], v51 offset:512\n"
    " s_waitcnt lgkmcnt(8)\n"
    " v_mfma_f32_16x16x32_bf16 v[12:15], v[76:79], v[72:75], v[12:15]\n"
    " v_mfma_f32_16x16x32_bf16 v[20:23], v[72:75], v[72:75], v[20:23]\n"
    " buffer_load_dwordx4 v[140:143], v1, s[12:15], s68 offen offset:3072 sc1\n"
    " ds_read_b64_tr_b16 v[68:69], v50 offset:4096\n"
    " ds_read_b64_tr_b16 v[70:71], v50 offset:4608\n"
    " s_waitcnt lgkmcnt(8)\n"
    " v_mfma_f32_16x16x32_bf16 v[8:11], v[76:79], v[52:55], v[8:11]\n"
    " v_mfma_f32_16x16x32_bf16 v[16:19], v[52:55], v[52:55], v[16:19]\n"
    " ds_read_b64_tr_b16 v[72:73], v51 offset:4096\n"
    " ds_read_b64_tr_b16 v[74:75], v51 offset:4608\n"
    " s_waitcnt lgkmcnt(8)\n"
    " v_mfma_f32_16x16x32_bf16 v[12:15], v[76:79], v[56:59], v[12:15]\n"
    " v_mfma_f32_16x16x32_bf16 v[20:23], v[56:59], v[56:59], v[20:23]\n"
    " s_nop 2\n"
    " v_cndmask_b32 v16, v16, v17, s[80:81]\n"
    " v_cndmask_b32 v18, v18, v19, s[80:81]\n"
    " v_cndmask_b32 v16, v16, v18, s[82:83]\n"
    " v_mul_f32 v40, s65, v8\n"
    " v_mul_f32 v16, s65, v16\n"
    " v_fma_f32 v16, -v40, v40, v16\n"
    " v_max_f32 v16, 0, v16\n"
    " v_add_f32 v16, 0x3727c5ac, v16\n"
    " v_rsq_f32 v41, v16\n"
    " v_cndmask_b32 v20, v20, v21, s[80:81]\n"
    " v_cndmask_b32 v22, v22, v23, s[80:81]\n"
    " v_cndmask_b32 v20, v20, v22, s[82:83]\n"
    " v_mul_f32 v42, s65, v12\n"
    " v_mul_f32 v20, s65, v20\n"
    " v_fma_f32 v20, -v42, v42, v20\n"
    " v_max_f32 v20, 0, v20\n"
    " v_add_f32 v20, 0x3727c5ac, v20\n"
    " v_rsq_f32 v43, v20\n"
    " v_mul_f32 v40, -v40, v41\n"
    " v_mul_f32 v42, -v42, v43\n"
    " s_mov_b64 exec, s[84:85]\n"
    " ds_write_b64 v7, v[40:41] offset:0\n"
    " ds_write_b64 v7, v[42:43] offset:128\n"
    " s_mov_b64 exec, -1\n"
    " s_waitcnt lgkmcnt(0)\n"
    " s_barrier\n"
    " s_add_u32 s68, s68, 4096\n"
    " buffer_load_dwordx4 v[144:147], v1, s[12:15], s68 offen offset:0 sc1\n"
    " ds_read_b64_tr_b16 v[52:53], v50 offset:8192\n"
    " ds_read_b64_tr_b16 v[54:55], v50 offset:8704\n"
    " v_mfma_f32_16x16x32_bf16 v[24:27], v[76:79], v[60:63], 0\n"
    " v_mfma_f32_16x16x32_bf16 v[32:35], v[60:63], v[60:63], 0\n"
    " ds_read_b64_tr_b16 v[56:57], v51 offset:8192\n"
    " ds_read_b64_tr_b16 v[58:59], v51 offset:8704\n"
    " v_mfma_f32_16x16x32_bf16 v[28:31], v[76:79], v[64:67], 0\n"
    " v_mfma_f32_16x16x32_bf16 v[36:39], v[64:67], v[64:67], 0\n"
    " buffer_load_dwordx4 v[148:151], v1, s[12:15], s68 offen offset:1024 sc1\n"
    " ds_read_b64_tr_b16 v[60:61], v50 offset:12288\n"
    " ds_read_b64_tr_b16 v[62:63], v50 offset:12800\n"
    " v_mfma_f32_16x16x32_bf16 v[24:27], v[76:79], v[68:71], v[24:27]\n"
    " v_mfma_f32_16x16x32_bf16 v[32:35], v[68:71], v[68:71], v[32:35]\n"
    " ds_read_b64_tr_b16 v[64:65], v51 offset:12288\n"
    " ds_read_b64_tr_b16 v[66:67], v51 offset:12800\n"
    " v_mfma_f32_16x16x32_bf16 v[28:31], v[76:79], v[72:75], v[28:31]\n"
    " v_mfma_f32_16x16x32_bf16 v[36:39], v[72:75], v[72:75], v[36:39]\n"
    " buffer_load_dwordx4 v[152:155], v1, s[12:15], s68 offen offset:2048 sc1\n"
    " buffer_load_dwordx4 v[156:159], v1, s[12:15], s68 offen offset:3072 sc1\n"
    " ds_read_b64_tr_b16 v[68:69], v50 offset:16384\n"
    " ds_read_b64_tr_b16 v[70:71], v50 offset:16896\n"
    " s_waitcnt lgkmcnt(8)\n"
    " v_mfma_f32_16x16x32_bf16 v[24:27], v[76:79], v[52:55], v[24:27]\n"
    " v_mfma_f32_16x16x32_bf16 v[32:35], v[52:55], v[52:55], v[32:35]\n"
    " ds_read_b64_tr_b16 v[72:73], v51 offset:16384\n"
    " ds_read_b64_tr_b16 v[74:75], v51 offset:16896\n"
    " s_waitcnt lgkmcnt(8)\n"
    " v_mfma_f32_16x16x32_bf16 v[28:31], v[76:79], v[56:59], v[28:31]\n"
    " v_mfma_f32_16x16x32_bf16 v[36:39], v[56:59], v[56:59], v[36:39]\n"
    " s_add_u32 s68, s68, 4096\n"
    " buffer_load_dwordx4 v[160:163], v1, s[12:15], s68 offen offset:0 sc1\n"
    " ds_read_b64_tr_b16 v[52:53], v50 offset:20480\n"
    " ds_read_b64_tr_b16 v[54:55], v50 offset:20992\n"
    " s_waitcnt lgkmcnt(8)\n"
    " v_mfma_f32_16x16x32_bf16 v[24:27], v[76:79], v[60:63], v[24:27]\n"
    " v_mfma_f32_16x16x32_bf16 v[32:35], v[60:63], v[60:63], v[32:35]\n"
    " ds_read_b64_tr_b16 v[56:57], v51 offset:20480\n"
    " ds_read_b64_tr_b16 v[58:59], v51 offset:20992\n"
    " s_waitcnt lgkmcnt(8)\n"
    " v_mfma_f32_16x16x32_bf16 v[28:31], v[76:79], v[64:67], v[28:31]\n"
    " v_mfma_f32_16x16x32_bf16 v[36:39], v[64:67], v[64:67], v[36:39]\n"
    " buffer_load_dwordx4 v[164:167], v1, s[12:15], s68 offen offset:1024 sc1\n"
    " buffer_load_dwordx4 v[168:171], v1, s[12:15], s68 offen offset:2048 sc1\n"
    " ds_read_b64_tr_b16 v[60:61], v50 offset:24576\n"
    " ds_read_b64_tr_b16 v[62:63], v50 offset:25088\n"
    " s_waitcnt lgkmcnt(8)\n"
    " v_mfma_f32_16x16x32_bf16 v[24:27], v[76:79], v[68:71], v[24:27]\n"
    " v_mfma_f32_16x16x32_bf16 v[32:35], v[68:71], v[68:71], v[32:35]\n"
    " ds_read_b64_tr_b16 v[64:65], v51 offset:24576\n"
    " ds_read_b64_tr_b16 v[66:67], v51 offset:25088\n"
    " s_waitcnt lgkmcnt(8)\n"
    " v_mfma_f32_16x16x32_bf16 v[28:31], v[76:79], v[72:75], v[28:31]\n"
    " v_mfma_f32_16x16x32_bf16 v[36:39], v[72:75], v[72:75], v[36:39]\n"
    " buffer_load_dwordx4 v[172:175], v1, s[12:15], s68 offen offset:3072 sc1\n"
    " ds_read_b64_tr_b16 v[68:69], v50 offset:28672\n"
    " ds_read_b64_tr_b16 v[70:71], v50 offset:29184\n"
    " s_waitcnt lgkmcnt(8)\n"
    " v_mfma_f32_16x16x32_bf16 v[24:27], v[76:79], v[52:55], v[24:27]\n"
    " v_mfma_f32_16x16x32_bf16 v[32:35], v[52:55], v[52:55], v[32:35]\n"
    " ds_read_b64_tr_b16 v[72:73], v51 offset:28672\n"
    " ds_read_b64_tr_b16 v[74:75], v51 offset:29184\n"
    " s_waitcnt lgkmcnt(8)\n"
    " v_mfma_f32_16x16x32_bf16 v[28:31], v[76:79], v[56:59], v[28:31]\n"
    " v_mfma_f32_16x16x32_bf16 v[36:39], v[56:59], v[56:59], v[36:39]\n"
    " s_add_u32 s68, s68, 4096\n"
    " buffer_load_dwordx4 v[176:179], v1, s[12:15], s68 offen offset:0 sc1\n"
    " buffer_load_dwordx4 v[180:183], v1, s[12:15], s68 offen offset:1024 sc1\n"
    " ds_read_b64_tr_b16 v[52:53], v50 offset:32768\n"
    " ds_read_b64_tr_b16 v[54:55], v50 offset:33280\n"
    " s_waitcnt lgkmcnt(8)\n"
    " v_mfma_f32_16x16x32_bf16 v[24:27], v[76:79], v[60:63], v[24:27]\n"
    " v_mfma_f32_16x16x32_bf16 v[32:35], v[60:63], v[60:63], v[32:35]\n"
    " ds_read_b64_tr_b16 v[56:57], v51 offset:32768\n"
    " ds_read_b64_tr_b16 v[58:59], v51 offset:33280\n"
    " s_waitcnt lgkmcnt(8)\n"
    " v_mfma_f32_16x16x32_bf16 v[28:31], v[76:79], v[64:67], v[28:31]\n"
    " v_mfma_f32_16x16x32_bf16 v[36:39], v[64:67], v[64:67], v[36:39]\n"
    " buffer_load_dwordx4 v[184:187], v1, s[12:15], s68 offen offset:2048 sc1\n"
    " ds_read_b64_tr_b16 v[60:61], v50 offset:36864\n"
    " ds_read_b64_tr_b16 v[62:63], v50 offset:37376\n"
    " s_waitcnt lgkmcnt(8)\n"
    " v_mfma_f32_16x16x32_bf16 v[24:27], v[76:79], v[68:71], v[24:27]\n"
    " v_mfma_f32_16x16x32_bf16 v[32:35], v[68:71], v[68:71], v[32:35]\n"
    " ds_read_b64_tr_b16 v[64:65], v51 offset:36864\n"
    " ds_read_b64_tr_b16 v[66:67], v51 offset:37376\n"
    " s_waitcnt lgkmcnt(8)\n"
    " v_mfma_f32_16x16x32_bf16 v[28:31], v[76:79], v[72:75], v[28:31]\n"
    " v_mfma_f32_16x16x32_bf16 v[36:39], v[72:75], v[72:75], v[36:39]\n"
    " buffer_load_dwordx4 v[188:191], v1, s[12:15], s68 offen offset:3072 sc1\n"
    " ds_read_b64_tr_b16 v[68:69], v50 offset:40960\n"
    " ds_read_b64_tr_b16 v[70:71], v50 offset:41472\n"
    " s_waitcnt lgkmcnt(8)\n"
    " v_mfma_f32_16x16x32_bf16 v[24:27], v[76:79], v[52:55], v[24:27]\n"
    " v_mfma_f32_16x16x32_bf16 v[32:35], v[52:55], v[52:55], v[32:35]\n"
    " ds_read_b64_tr_b16 v[72:73], v51 offset:40960\n"
    " ds_read_b64_tr_b16 v[74:75], v51 offset:41472\n"
    " s_waitcnt lgkmcnt(8)\n"
    " v_mfma_f32_16x16x32_bf16 v[28:31], v[76:79], v[56:59], v[28:31]\n"
    " v_mfma_f32_16x16x32_bf16 v[36:39], v[56:59], v[56:59], v[36:39]\n"
    " ds_read_b64_tr_b16 v[52:53], v50 offset:45056\n"
    " ds_read_b64_tr_b16 v[54:55], v50 offset:45568\n"
    " s_waitcnt lgkmcnt(8)\n"
    " v_mfma_f32_16x16x32_bf16 v[24:27], v[76:79], v[60:63], v[24:27]\n"
    " v_mfma_f32_16x16x32_bf16 v[32:35], v[60:63], v[60:63], v[32:35]\n"
    " ds_read_b64_tr_b16 v[56:57], v51 offset:45056\n"
    " ds_read_b64_tr_b16 v[58:59], v51 offset:45568\n"
    " s_waitcnt lgkmcnt(8)\n"
    " v_mfma_f32_16x16x32_bf16 v[28:31], v[76:79], v[64:67], v[28:31]\n"
    " v_mfma_f32_16x16x32_bf16 v[36:39], v[64:67], v[64:67], v[36:39]\n"
    " ds_read_b64_tr_b16 v[60:61], v50 offset:49152\n"
    " ds_read_b64_tr_b16 v[62:63], v50 offset:49664\n"
    " s_waitcnt lgkmcnt(8)\n"
    " v_mfma_f32_16x16x32_bf16 v[24:27], v[76:79], v[68:71], v[24:27]\n"
    " v_mfma_f32_16x16x32_bf16 v[32:35], v[68:71], v[68:71], v[32:35]\n"
    " ds_read_b64_tr_b16 v[64:65], v51 offset:49152\n"
    " ds_read_b64_tr_b16 v[66:67], v51 offset:49664\n"
    " s_waitcnt lgkmcnt(8)\n"
    " v_mfma_f32_16x16x32_bf16 v[28:31], v[76:79], v[72:75], v[28:31]\n"
    " v_mfma_f32_16x16x32_bf16 v[36:39], v[72:75], v[72:75], v[36:39]\n"
    " ds_read_b64_tr_b16 v[68:69], v50 offset:53248\n"
    " ds_read_b64_tr_b16 v[70:71], v50 offset:53760\n"
    " s_waitcnt lgkmcnt(8)\n"
    " v_mfma_f32_16x16x32_bf16 v[24:27], v[76:79], v[52:55], v[24:27]\n"
    " v_mfma_f32_16x16x32_bf16 v[32:35], v[52:55], v[52:55], v[32:35]\n"
    " ds_read_b64_tr_b16 v[72:73], v51 offset:53248\n"
    " ds_read_b64_tr_b16 v[74:75], v51 offset:53760\n"
    " s_waitcnt lgkmcnt(8)\n"
    " v_mfma_f32_16x16x32_bf16 v[28:31], v[76:79], v[56:59], v[28:31]\n"
    " v_mfma_f32_16x16x32_bf16 v[36:39], v[56:59], v[56:59], v[36:39]\n"
    " ds_read_b64_tr_b16 v[52:53], v50 offset:57344\n"
    " ds_read_b64_tr_b16 v[54:55], v50 offset:57856\n"
    " s_waitcnt lgkmcnt(8)\n"
    " v_mfma_f32_16x16x32_bf16 v[24:27], v[76:79], v[60:63], v[24:27]\n"
    " v_mfma_f32_16x16x32_bf16 v[32:35], v[60:63], v[60:63], v[32:35]\n"
    " ds_read_b64_tr_b16 v[56:57], v51 offset:57344\n"
    " ds_read_b64_tr_b16 v[58:59], v51 offset:57856\n"
    " s_waitcnt lgkmcnt(8)\n"
    " v_mfma_f32_16x16x32_bf16 v[28:31], v[76:79], v[64:67], v[28:31]\n"
    " v_mfma_f32_16x16x32_bf16 v[36:39], v[64:67], v[64:67], v[36:39]\n"
    " ds_read_b64_tr_b16 v[60:61], v50 offset:61440\n"
    " ds_read_b64_tr_b16 v[62:63], v50 offset:61952\n"
    " s_waitcnt lgkmcnt(8)\n"
    " v_mfma_f32_16x16x32_bf16 v[24:27], v[76:79], v[68:71], v[24:27]\n"
    " v_mfma_f32_16x16x32_bf16 v[32:35], v[68:71], v[68:71], v[32:35]\n"
    " ds_read_b64_tr_b16 v[64:65], v51 offset:61440\n"
    " ds_read_b64_tr_b16 v[66:67], v51 offset:61952\n"
    " s_waitcnt lgkmcnt(8)\n"
    " v_mfma_f32_16x16x32_bf16 v[28:31], v[76:79], v[72:75], v[28:31]\n"
    " v_mfma_f32_16x16x32_bf16 v[36:39], v[72:75], v[72:75], v[36:39]\n"
    " s_waitcnt lgkmcnt(6)\n"
    " v_mfma_f32_16x16x32_bf16 v[24:27], v[76:79], v[52:55], v[24:27]\n"
    " v_mfma_f32_16x16x32_bf16 v[32:35], v[52:55], v[52:55], v[32:35]\n"
    " s_waitcnt lgkmcnt(4)\n"
    " v_mfma_f32_16x16x32_bf16 v[28:31], v[76:79], v[56:59], v[28:31]\n"
    " v_mfma_f32_16x16x32_bf16 v[36:39], v[56:59], v[56:59], v[36:39]\n"
    " s_waitcnt lgkmcnt(2)\n"
    " v_mfma_f32_16x16x32_bf16 v[24:27], v[76:79], v[60:63], v[24:27]\n"
    " v_mfma_f32_16x16x32_bf16 v[32:35], v[60:63], v[60:63], v[32:35]\n"
    " s_waitcnt lgkmcnt(0)\n"
    " v_mfma_f32_16x16x32_bf16 v[28:31], v[76:79], v[64:67], v[28:31]\n"
    " v_mfma_f32_16x16x32_bf16 v[36:39], v[64:67], v[64:67], v[36:39]\n"
    " s_nop 4\n"
    " v_cndmask_b32 v32, v32, v33, s[80:81]\n"
    " v_cndmask_b32 v34, v34, v35, s[80:81]\n"
    " v_cndmask_b32 v32, v32, v34, s[82:83]\n"
    " v_mul_f32 v40, s65, v24\n"
    " v_mul_f32 v32, s65, v32\n"
    " v_fma_f32 v32, -v40, v40, v32\n"
    " v_max_f32 v32, 0, v32\n"
    " v_add_f32 v32, 0x3727c5ac, v32\n"
    " v_rsq_f32 v41, v32\n"
    " v_cndmask_b32 v36, v36, v37, s[80:81]\n"
    " v_cndmask_b32 v38, v38, v39, s[80:81]\n"
    " v_cndmask_b32 v36, v36, v38, s[82:83]\n"
    " v_mul_f32 v42, s65, v28\n"
    " v_mul_f32 v36, s65, v36\n"
    " v_fma_f32 v36, -v42, v42, v36\n"
    " v_max_f32 v36, 0, v36\n"
    " v_add_f32 v36, 0x3727c5ac, v36\n"
    " v_rsq_f32 v43, v36\n"
    " v_mul_f32 v40, -v40, v41\n"
    " v_mul_f32 v42, -v42, v43\n"
    " s_mov_b64 exec, s[84:85]\n"
    " ds_write_b64 v7, v[40:41] offset:256\n"
    " ds_write_b64 v7, v[42:43] offset:384\n"
    " s_mov_b64 exec, -1\n"
    " v_bfe_u32 v5, v0, 2, 4\n"
    " v_and_b32 v6, 3, v0\n"
    " v_lshlrev_b32 v6, 3, v6\n"
    " v_lshl_add_u32 v5, v5, 7, v6\n"
    " v_bfe_u32 v6, v0, 3, 1\n"
    " v_lshl_add_u32 v5, v6, 5, v5\n"
    " v_bfe_u32 v6, v0, 5, 1\n"
    " v_lshl_add_u32 v5, v6, 6, v5\n"
    " s_lshl_b32 s64, s49, 13\n"
    " v_add_u32 v48, s64, v5\n"
    " v_xor_b32 v49, 32, v48\n"
    " v_xor_b32 v50, 64, v48\n"
    " v_xor_b32 v51, 96, v48\n"
    " ds_read_b64_tr_b16 v[8:9], v48 offset:0\n"
    " ds_read_b64_tr_b16 v[10:11], v48 offset:2048\n"
    " ds_read_b64_tr_b16 v[12:13], v48 offset:4096\n"
    " ds_read_b64_tr_b16 v[14:15], v48 offset:6144\n"
    " ds_read_b64_tr_b16 v[16:17], v49 offset:0\n"
    " ds_read_b64_tr_b16 v[18:19], v49 offset:2048\n"
    " ds_read_b64_tr_b16 v[20:21], v49 offset:4096\n"
    " ds_read_b64_tr_b16 v[22:23], v49 offset:6144\n"
    " ds_read_b64_tr_b16 v[24:25], v50 offset:0\n"
    " ds_read_b64_tr_b16 v[26:27], v50 offset:2048\n"
    " ds_read_b64_tr_b16 v[28:29], v50 offset:4096\n"
    " ds_read_b64_tr_b16 v[30:31], v50 offset:6144\n"
    " ds_read_b64_tr_b16 v[32:33], v51 offset:0\n"
    " ds_read_b64_tr_b16 v[34:35], v51 offset:2048\n"
    " ds_read_b64_tr_b16 v[36:37], v51 offset:4096\n"
    " ds_read_b64_tr_b16 v[38:39], v51 offset:6144\n"
    " s_waitcnt lgkmcnt(0)\n"
    " s_barrier\n"
    " s_setprio 0\n"
    " buffer_load_dwordx4 v[224:227], v1, s[12:15], s72 offen offset:0 sc1\n"
    " buffer_load_dwordx4 v[228:231], v1, s[12:15], s72 offen offset:1024 sc1\n"
    " buffer_load_dwordx4 v[232:235], v1, s[12:15], s72 offen offset:2048 sc1\n"
    " buffer_load_dwordx4 v[236:239], v1, s[12:15], s72 offen offset:3072 sc1\n"
    " buffer_load_dwordx4 v[240:243], v1, s[12:15], s73 offen offset:0 sc1\n"
    " buffer_load_dwordx4 v[244:247], v1, s[12:15], s73 offen offset:1024 sc1\n"
    " buffer_load_dwordx4 v[248:251], v1, s[12:15], s73 offen offset:2048 sc1\n"
    " s_lshl_b32 s60, s44, 13\n"
    " s_add_u32 s60, s60, 66560\n"
    " v_lshrrev_b32 v5, 4, v1\n"
    " v_and_b32 v5, 32, v5\n"
    " v_xor_b32 v5, v5, v1\n"
    " v_add_u32 v48, s60, v5\n"
    " v_xor_b32 v49, 64, v48\n"
    " v_xor_b32 v50, 128, v48\n"
    " v_xor_b32 v51, 192, v48\n"
    " s_waitcnt vmcnt(6)\n"
    " v_bitop3_b32 v2, v224, v227, s88 bitop3:0x7e\n"
    " v_bitop3_b32 v3, v225, v226, s89 bitop3:0x7e\n"
    " v_bitop3_b32 v2, v2, v3, s90 bitop3:0xa8\n"
    " v_cmp_ne_u32 vcc, 0, v2\n"
    " v_lshlrev_b32 v192, 16, v8\n"
    " v_and_b32 v193, s53, v8\n"
    " v_lshlrev_b32 v194, 16, v9\n"
    " v_and_b32 v195, s53, v9\n"
    " v_lshlrev_b32 v196, 16, v10\n"
    " v_and_b32 v197, s53, v10\n"
    " v_lshlrev_b32 v198, 16, v11\n"
    " v_and_b32 v199, s53, v11\n"
    " v_lshlrev_b32 v200, 16, v12\n"
    " v_and_b32 v201, s53, v12\n"
    " v_lshlrev_b32 v202, 16, v13\n"
    " v_and_b32 v203, s53, v13\n"
    " v_lshlrev_b32 v204, 16, v14\n"
    " v_and_b32 v205, s53, v14\n"
    " v_lshlrev_b32 v206, 16, v15\n"
    " v_and_b32 v207, s53, v15\n"
    " s_cbranch_vccnz .Lgs_31\n"
    ".Lgsb_32:\n"
    " buffer_load_dwordx4 v[252:255], v1, s[12:15], s74 offen offset:0 sc1\n"
    " v_mfma_f32_16x16x32_bf16 v[192:195], v[80:83], v[224:227], v[192:195]\n"
    " v_mfma_f32_16x16x32_bf16 v[196:199], v[84:87], v[224:227], v[196:199]\n"
    " s_waitcnt vmcnt(6)\n"
    " v_bitop3_b32 v2, v228, v231, s88 bitop3:0x7e\n"
    " v_mfma_f32_16x16x32_bf16 v[200:203], v[88:91], v[224:227], v[200:203]\n"
    " v_bitop3_b32 v3, v229, v230, s89 bitop3:0x7e\n"
    " v_mfma_f32_16x16x32_bf16 v[204:207], v[92:95], v[224:227], v[204:207]\n"
    " v_bitop3_b32 v2, v2, v3, s90 bitop3:0xa8\n"
    " v_cmp_ne_u32 vcc, 0, v2\n"
    " s_cbranch_vccnz .Lgs_34\n"
    ".Lgsb_35:\n"
    " buffer_load_dwordx4 v[52:55], v1, s[12:15], s74 offen offset:1024 sc1\n"
    " v_mfma_f32_16x16x32_bf16 v[192:195], v[96:99], v[228:231], v[192:195]\n"
    " v_mfma_f32_16x16x32_bf16 v[196:199], v[100:103], v[228:231], v[196:199]\n"
    " s_waitcnt vmcnt(6)\n"
    " v_bitop3_b32 v2, v232, v235, s88 bitop3:0x7e\n"
    " v_mfma_f32_16x16x32_bf16 v[200:203], v[104:107], v[228:231], v[200:203]\n"
    " v_bitop3_b32 v3, v233, v234, s89 bitop3:0x7e\n"
    " v_mfma_f32_16x16x32_bf16 v[204:207], v[108:111], v[228:231], v[204:207]\n"
    " v_bitop3_b32 v2, v2, v3, s90 bitop3:0xa8\n"
    " v_cmp_ne_u32 vcc, 0, v2\n"
    " s_cbranch_vccnz .Lgs_37\n"
    ".Lgsb_38:\n"
    " buffer_load_dwordx4 v[56:59], v1, s[12:15], s74 offen offset:2048 sc1\n"
    " v_mfma_f32_16x16x32_bf16 v[192:195], v[112:115], v[232:235], v[192:195]\n"
    " v_mfma_f32_16x16x32_bf16 v[196:199], v[116:119], v[232:235], v[196:199]\n"
    " s_waitcnt vmcnt(6)\n"
    " v_bitop3_b32 v2, v236, v239, s88 bitop3:0x7e\n"
    " v_mfma_f32_16x16x32_bf16 v[200:203], v[120:123], v[232:235], v[200:203]\n"
    " v_bitop3_b32 v3, v237, v238, s89 bitop3:0x7e\n"
    " v_mfma_f32_16x16x32_bf16 v[204:207], v[124:127], v[232:235], v[204:207]\n"
    " v_bitop3_b32 v2, v2, v3, s90 bitop3:0xa8\n"
    " v_cmp_ne_u32 vcc, 0, v2\n"
    " s_cbranch_vccnz .Lgs_40\n"
    ".Lgsb_41:\n"
    " buffer_load_dwordx4 v[60:63], v1, s[12:15], s74 offen offset:3072 sc1\n"
    " v_mfma_f32_16x16x32_bf16 v[192:195], v[128:131], v[236:239], v[192:195]\n"
    " v_mfma_f32_16x16x32_bf16 v[196:199], v[132:135], v[236:239], v[196:199]\n"
    " s_waitcnt vmcnt(6)\n"
    " v_bitop3_b32 v2, v240, v243, s88 bitop3:0x7e\n"
    " v_mfma_f32_16x16x32_bf16 v[200:203], v[136:139], v[236:239], v[200:203]\n"
    " v_bitop3_b32 v3, v241, v242, s89 bitop3:0x7e\n"
    " v_mfma_f32_16x16x32_bf16 v[204:207], v[140:143], v[236:239], v[204:207]\n"
    " v_bitop3_b32 v2, v2, v3, s90 bitop3:0xa8\n"
    " v_cmp_ne_u32 vcc, 0, v2\n"
    " s_cbranch_vccnz .Lgs_43\n"
    ".Lgsb_44:\n"
    " buffer_load_dwordx4 v[64:67], v1, s[12:15], s75 offen offset:0 sc1\n"
    " v_mfma_f32_16x16x32_bf16 v[192:195], v[144:147], v[240:243], v[192:195]\n"
    " v_mfma_f32_16x16x32_bf16 v[196:199], v[148:151], v[240:243], v[196:199]\n"
    " s_waitcnt vmcnt(6)\n"
    " v_bitop3_b32 v2, v244, v247, s88 bitop3:0x7e\n"
    " v_mfma_f32_16x16x32_bf16 v[200:203], v[152:155], v[240:243], v[200:203]\n"
    " v_bitop3_b32 v3, v245, v246, s89 bitop3:0x7e\n"
    " v_mfma_f32_16x16x32_bf16 v[204:207], v[156:159], v[240:243], v[204:207]\n"
    " v_bitop3_b32 v2, v2, v3, s90 bitop3:0xa8\n"
    " v_cmp_ne_u32 vcc, 0, v2\n"
    " s_cbranch_vccnz .Lgs_46\n"
    ".Lgsb_47:\n"
    " buffer_load_dwordx4 v[68:71], v1, s[12:15], s75 offen offset:1024 sc1\n"
    " v_mfma_f32_16x16x32_bf16 v[192:195], v[160:163], v[244:247], v[192:195]\n"
    " v_mfma_f32_16x16x32_bf16 v[196:199], v[164:167], v[244:247], v[196:199]\n"
    " s_waitcnt vmcnt(6)\n"
    " v_bitop3_b32 v2, v248, v251, s88 bitop3:0x7e\n"
    " v_mfma_f32_16x16x32_bf16 v[200:203], v[168:171], v[244:247], v[200:203]\n"
    " v_bitop3_b32 v3, v249, v250, s89 bitop3:0x7e\n"
    " v_mfma_f32_16x16x32_bf16 v[204:207], v[172:175], v[244:247], v[204:207]\n"
    " v_bitop3_b32 v2, v2, v3, s90 bitop3:0xa8\n"
    " v_cmp_ne_u32 vcc, 0, v2\n"
    " s_cbranch_vccnz .Lgs_49\n"
    ".Lgsb_50:\n"
    " buffer_load_dwordx4 v[72:75], v1, s[12:15], s75 offen offset:2048 sc1\n"
    " v_mfma_f32_16x16x32_bf16 v[192:195], v[176:179], v[248:251], v[192:195]\n"
    " v_mfma_f32_16x16x32_bf16 v[196:199], v[180:183], v[248:251], v[196:199]\n"
    " s_waitcnt vmcnt(6)\n"
    " v_bitop3_b32 v2, v252, v255, s88 bitop3:0x7e\n"
    " v_mfma_f32_16x16x32_bf16 v[200:203], v[184:187], v[248:251], v[200:203]\n"
    " v_bitop3_b32 v3, v253, v254, s89 bitop3:0x7e\n"
    " v_mfma_f32_16x16x32_bf16 v[204:207], v[188:191], v[248:251], v[204:207]\n"
    " v_bitop3_b32 v2, v2, v3, s90 bitop3:0xa8\n"
    " v_cmp_ne_u32 vcc, 0, v2\n"
    " v_lshlrev_b32 v208, 16, v16\n"
    " v_and_b32 v209, s53, v16\n"
    " v_lshlrev_b32 v210, 16, v17\n"
    " v_and_b32 v211, s53, v17\n"
    " v_lshlrev_b32 v212, 16, v18\n"
    " v_and_b32 v213, s53, v18\n"
    " v_lshlrev_b32 v214, 16, v19\n"
    " v_and_b32 v215, s53, v19\n"
    " v_lshlrev_b32 v216, 16, v20\n"
    " v_and_b32 v217, s53, v20\n"
    " v_lshlrev_b32 v218, 16, v21\n"
    " v_and_b32 v219, s53, v21\n"
    " v_lshlrev_b32 v220, 16, v22\n"
    " v_and_b32 v221, s53, v22\n"
    " v_lshlrev_b32 v222, 16, v23\n"
    " v_and_b32 v223, s53, v23\n"
    " s_cbranch_vccnz .Lgs_52\n"
    ".Lgsb_53:\n"
    " buffer_load_dwordx4 v[224:227], v1, s[12:15], s76 offen offset:0 sc1\n"
    " v_mfma_f32_16x16x32_bf16 v[208:211], v[80:83], v[252:255], v[208:211]\n"
    " v_mfma_f32_16x16x32_bf16 v[212:215], v[84:87], v[252:255], v[212:215]\n"
    " s_waitcnt vmcnt(6)\n"
    " v_bitop3_b32 v2, v52, v55, s88 bitop3:0x7e\n"
    " v_mfma_f32_16x16x32_bf16 v[216:219], v[88:91], v[252:255], v[216:219]\n"
    " v_bitop3_b32 v3, v53, v54, s89 bitop3:0x7e\n"
    " v_mfma_f32_16x16x32_bf16 v[220:223], v[92:95], v[252:255], v[220:223]\n"
    " v_bitop3_b32 v2, v2, v3, s90 bitop3:0xa8\n"
    " v_cmp_ne_u32 vcc, 0, v2\n"
    " s_cbranch_vccnz .Lgs_55\n"
    ".Lgsb_56:\n"
    " buffer_load_dwordx4 v[228:231], v1, s[12:15], s76 offen offset:1024 sc1\n"
    " v_mfma_f32_16x16x32_bf16 v[208:211], v[96:99], v[52:55], v[208:211]\n"
    " v_mfma_f32_16x16x32_bf16 v[212:215], v[100:103], v[52:55], v[212:215]\n"
    " s_waitcnt vmcnt(6)\n"
    " v_bitop3_b32 v2, v56, v59, s88 bitop3:0x7e\n"
    " v_mfma_f32_16x16x32_bf16 v[216:219], v[104:107], v[52:55], v[216:219]\n"
    " v_bitop3_b32 v3, v57, v58, s89 bitop3:0x7e\n"
    " v_mfma_f32_16x16x32_bf16 v[220:223], v[108:111], v[52:55], v[220:223]\n"
    " v_bitop3_b32 v2, v2, v3, s90 bitop3:0xa8\n"
    " v_cmp_ne_u32 vcc, 0, v2\n"
    " s_cbranch_vccnz .Lgs_58\n"
    ".Lgsb_59:\n"
    " buffer_load_dwordx4 v[232:235], v1, s[12:15], s76 offen offset:2048 sc1\n"
    " v_mfma_f32_16x16x32_bf16 v[208:211], v[112:115], v[56:59], v[208:211]\n"
    " ds_write_b128 v48, v[192:195] offset:0\n"
    " v_mfma_f32_16x16x32_bf16 v[212:215], v[116:119], v[56:59], v[212:215]\n"
    " s_waitcnt vmcnt(6)\n"
    " v_bitop3_b32 v2, v60, v63, s88 bitop3:0x7e\n"
    " v_mfma_f32_16x16x32_bf16 v[216:219], v[120:123], v[56:59], v[216:219]\n"
    " v_bitop3_b32 v3, v61, v62, s89 bitop3:0x7e\n"
    " v_mfma_f32_16x16x32_bf16 v[220:223], v[124:127], v[56:59], v[220:223]\n"
    " v_bitop3_b32 v2, v2, v3, s90 bitop3:0xa8\n"
    " v_cmp_ne_u32 vcc, 0, v2\n"
    " s_cbranch_vccnz .Lgs_61\n"
    ".Lgsb_62:\n"
    " buffer_load_dwordx4 v[236:239], v1, s[12:15], s76 offen offset:3072 sc1\n"
    " v_mfma_f32_16x16x32_bf16 v[208:211], v[128:131], v[60:63], v[208:211]\n"
    " ds_write_b128 v49, v[196:199] offset:1024\n"
    " v_mfma_f32_16x16x32_bf16 v[212:215], v[132:135], v[60:63], v[212:215]\n"
    " s_waitcnt vmcnt(6)\n"
    " v_bitop3_b32 v2, v64, v67, s88 bitop3:0x7e\n"
    " v_mfma_f32_16x16x32_bf16 v[216:219], v[136:139], v[60:63], v[216:219]\n"
    " v_bitop3_b32 v3, v65, v66, s89 bitop3:0x7e\n"
    " v_mfma_f32_16x16x32_bf16 v[220:223], v[140:143], v[60:63], v[220:223]\n"
    " v_bitop3_b32 v2, v2, v3, s90 bitop3:0xa8\n"
    " v_cmp_ne_u32 vcc, 0, v2\n"
    " s_cbranch_vccnz .Lgs_64\n"
    ".Lgsb_65:\n"
    " buffer_load_dwordx4 v[240:243], v1, s[12:15], s77 offen offset:0 sc1\n"
    " v_mfma_f32_16x16x32_bf16 v[208:211], v[144:147], v[64:67], v[208:211]\n"
    " ds_write_b128 v50, v[200:203] offset:2048\n"
    " v_mfma_f32_16x16x32_bf16 v[212:215], v[148:151], v[64:67], v[212:215]\n"
    " s_waitcnt vmcnt(6)\n"
    " v_bitop3_b32 v2, v68, v71, s88 bitop3:0x7e\n"
    " v_mfma_f32_16x16x32_bf16 v[216:219], v[152:155], v[64:67], v[216:219]\n"
    " v_bitop3_b32 v3, v69, v70, s89 bitop3:0x7e\n"
    " v_mfma_f32_16x16x32_bf16 v[220:223], v[156:159], v[64:67], v[220:223]\n"
    " v_bitop3_b32 v2, v2, v3, s90 bitop3:0xa8\n"
    " v_cmp_ne_u32 vcc, 0, v2\n"
    " s_cbranch_vccnz .Lgs_67\n"
    ".Lgsb_68:\n"
    " buffer_load_dwordx4 v[244:247], v1, s[12:15], s77 offen offset:1024 sc1\n"
    " v_mfma_f32_16x16x32_bf16 v[208:211], v[160:163], v[68:71], v[208:211]\n"
    " ds_write_b128 v51, v[204:207] offset:3072\n"
    " v_mfma_f32_16x16x32_bf16 v[212:215], v[164:167], v[68:71], v[212:215]\n"
    " s_waitcnt vmcnt(6)\n"
    " v_bitop3_b32 v2, v72, v75, s88 bitop3:0x7e\n"
    " v_mfma_f32_16x16x32_bf16 v[216:219], v[168:171], v[68:71], v[216:219]\n"
    " v_bitop3_b32 v3, v73, v74, s89 bitop3:0x7e\n"
    " v_mfma_f32_16x16x32_bf16 v[220:223], v[172:175], v[68:71], v[220:223]\n"
    " v_bitop3_b32 v2, v2, v3, s90 bitop3:0xa8\n"
    " v_cmp_ne_u32 vcc, 0, v2\n"
    " s_cbranch_vccnz .Lgs_70\n"
    ".Lgsb_71:\n"
    " buffer_load_dwordx4 v[248:251], v1, s[12:15], s77 offen offset:2048 sc1\n"
    " v_mfma_f32_16x16x32_bf16 v[208:211], v[176:179], v[72:75], v[208:211]\n"
    " v_mfma_f32_16x16x32_bf16 v[212:215], v[180:183], v[72:75], v[212:215]\n"
    " s_waitcnt vmcnt(6)\n"
    " v_bitop3_b32 v2, v224, v227, s88 bitop3:0x7e\n"
    " v_mfma_f32_16x16x32_bf16 v[216:219], v[184:187], v[72:75], v[216:219]\n"
    " v_bitop3_b32 v3, v225, v226, s89 bitop3:0x7e\n"
    " v_mfma_f32_16x16x32_bf16 v[220:223], v[188:191], v[72:75], v[220:223]\n"
    " v_bitop3_b32 v2, v2, v3, s90 bitop3:0xa8\n"
    " v_cmp_ne_u32 vcc, 0, v2\n"
    " v_lshlrev_b32 v192, 16, v24\n"
    " v_and_b32 v193, s53, v24\n"
    " v_lshlrev_b32 v194, 16, v25\n"
    " v_and_b32 v195, s53, v25\n"
    " v_lshlrev_b32 v196, 16, v26\n"
    " v_and_b32 v197, s53, v26\n"
    " v_lshlrev_b32 v198, 16, v27\n"
    " v_and_b32 v199, s53, v27\n"
    " v_lshlrev_b32 v200, 16, v28\n"
    " v_and_b32 v201, s53, v28\n"
    " v_lshlrev_b32 v202, 16, v29\n"
    " v_and_b32 v203, s53, v29\n"
    " v_lshlrev_b32 v204, 16, v30\n"
    " v_and_b32 v205, s53, v30\n"
    " v_lshlrev_b32 v206, 16, v31\n"
    " v_and_b32 v207, s53, v31\n"
    " s_cbranch_vccnz .Lgs_73\n"
    ".Lgsb_74:\n"
    " buffer_load_dwordx4 v[252:255], v1, s[12:15], s78 offen offset:0 sc1\n"
    " v_mfma_f32_16x16x32_bf16 v[192:195], v[80:83], v[224:227], v[192:195]\n"
    " v_mfma_f32_16x16x32_bf16 v[196:199], v[84:87], v[224:227], v[196:199]\n"
    " s_waitcnt vmcnt(6)\n"
    " v_bitop3_b32 v2, v228, v231, s88 bitop3:0x7e\n"
    " v_mfma_f32_16x16x32_bf16 v[200:203], v[88:91], v[224:227], v[200:203]\n"
    " v_bitop3_b32 v3, v229, v230, s89 bitop3:0x7e\n"
    " v_mfma_f32_16x16x32_bf16 v[204:207], v[92:95], v[224:227], v[204:207]\n"
    " v_bitop3_b32 v2, v2, v3, s90 bitop3:0xa8\n"
    " v_cmp_ne_u32 vcc, 0, v2\n"
    " s_cbranch_vccnz .Lgs_76\n"
    ".Lgsb_77:\n"
    " buffer_load_dwordx4 v[52:55], v1, s[12:15], s78 offen offset:1024 sc1\n"
    " v_mfma_f32_16x16x32_bf16 v[192:195], v[96:99], v[228:231], v[192:195]\n"
    " v_mfma_f32_16x16x32_bf16 v[196:199], v[100:103], v[228:231], v[196:199]\n"
    " s_waitcnt vmcnt(6)\n"
    " v_bitop3_b32 v2, v232, v235, s88 bitop3:0x7e\n"
    " v_mfma_f32_16x16x32_bf16 v[200:203], v[104:107], v[228:231], v[200:203]\n"
    " v_bitop3_b32 v3, v233, v234, s89 bitop3:0x7e\n"
    " v_mfma_f32_16x16x32_bf16 v[204:207], v[108:111], v[228:231], v[204:207]\n"
    " v_bitop3_b32 v2, v2, v3, s90 bitop3:0xa8\n"
    " v_cmp_ne_u32 vcc, 0, v2\n"
    " s_cbranch_vccnz .Lgs_79\n"
    ".Lgsb_80:\n"
    " buffer_load_dwordx4 v[56:59], v1, s[12:15], s78 offen offset:2048 sc1\n"
    " v_mfma_f32_16x16x32_bf16 v[192:195], v[112:115], v[232:235], v[192:195]\n"
    " ds_write_b128 v48, v[208:211] offset:4096\n"
    " v_mfma_f32_16x16x32_bf16 v[196:199], v[116:119], v[232:235], v[196:199]\n"
    " s_waitcnt vmcnt(6)\n"
    " v_bitop3_b32 v2, v236, v239, s88 bitop3:0x7e\n"
    " v_mfma_f32_16x16x32_bf16 v[200:203], v[120:123], v[232:235], v[200:203]\n"
    " v_bitop3_b32 v3, v237, v238, s89 bitop3:0x7e\n"
    " v_mfma_f32_16x16x32_bf16 v[204:207], v[124:127], v[232:235], v[204:207]\n"
    " v_bitop3_b32 v2, v2, v3, s90 bitop3:0xa8\n"
    " v_cmp_ne_u32 vcc, 0, v2\n"
    " s_cbranch_vccnz .Lgs_82\n"
    ".Lgsb_83:\n"
    " buffer_load_dwordx4 v[60:63], v1, s[12:15], s78 offen offset:3072 sc1\n"
    " v_mfma_f32_16x16x32_bf16 v[192:195], v[128:131], v[236:239], v[192:195]\n"
    " ds_write_b128 v49, v[212:215] offset:5120\n"
    " v_mfma_f32_16x16x32_bf16 v[196:199], v[132:135], v[236:239], v[196:199]\n"
    " s_waitcnt vmcnt(6)\n"
    " v_bitop3_b32 v2, v240, v243, s88 bitop3:0x7e\n"
    " v_mfma_f32_16x16x32_bf16 v[200:203], v[136:139], v[236:239], v[200:203]\n"
    " v_bitop3_b32 v3, v241, v242, s89 bitop3:0x7e\n"
    " v_mfma_f32_16x16x32_bf16 v[204:207], v[140:143], v[236:239], v[204:207]\n"
    " v_bitop3_b32 v2, v2, v3, s90 bitop3:0xa8\n"
    " v_cmp_ne_u32 vcc, 0, v2\n"
    " s_cbranch_vccnz .Lgs_85\n"
    ".Lgsb_86:\n"
    " buffer_load_dwordx4 v[64:67], v1, s[12:15], s79 offen offset:0 sc1\n"
    " v_mfma_f32_16x16x32_bf16 v[192:195], v[144:147], v[240:243], v[192:195]\n"
    " ds_write_b128 v50, v[216:219] offset:6144\n"
    " v_mfma_f32_16x16x32_bf16 v[196:199], v[148:151], v[240:243], v[196:199]\n"
    " s_waitcnt vmcnt(6)\n"
    " v_bitop3_b32 v2, v244, v247, s88 bitop3:0x7e\n"
    " v_mfma_f32_16x16x32_bf16 v[200:203], v[152:155], v[240:243], v[200:203]\n"
    " v_bitop3_b32 v3, v245, v246, s89 bitop3:0x7e\n"
    " v_mfma_f32_16x16x32_bf16 v[204:207], v[156:159], v[240:243], v[204:207]\n"
    " v_bitop3_b32 v2, v2, v3, s90 bitop3:0xa8\n"
    " v_cmp_ne_u32 vcc, 0, v2\n"
    " s_cbranch_vccnz .Lgs_88\n"
    ".Lgsb_89:\n"
    " buffer_load_dwordx4 v[68:71], v1, s[12:15], s79 offen offset:1024 sc1\n"
    " v_mfma_f32_16x16x32_bf16 v[192:195], v[160:163], v[244:247], v[192:195]\n"
    " ds_write_b128 v51, v[220:223] offset:7168\n"
    " v_mfma_f32_16x16x32_bf16 v[196:199], v[164:167], v[244:247], v[196:199]\n"
    " s_waitcnt vmcnt(6)\n"
    " v_bitop3_b32 v2, v248, v251, s88 bitop3:0x7e\n"
    " v_mfma_f32_16x16x32_bf16 v[200:203], v[168:171], v[244:247], v[200:203]\n"
    " v_bitop3_b32 v3, v249, v250, s89 bitop3:0x7e\n"
    " v_mfma_f32_16x16x32_bf16 v[204:207], v[172:175], v[244:247], v[204:207]\n"
    " v_bitop3_b32 v2, v2, v3, s90 bitop3:0xa8\n"
    " v_cmp_ne_u32 vcc, 0, v2\n"
    " s_cbranch_vccnz .Lgs_91\n"
    ".Lgsb_92:\n"
    " buffer_load_dwordx4 v[72:75], v1, s[12:15], s79 offen offset:2048 sc1\n"
    " v_mfma_f32_16x16x32_bf16 v[192:195], v[176:179], v[248:251], v[192:195]\n"
    " v_mfma_f32_16x16x32_bf16 v[196:199], v[180:183], v[248:251], v[196:199]\n"
    " s_waitcnt vmcnt(6)\n"
    " v_bitop3_b32 v2, v252, v255, s88 bitop3:0x7e\n"
    " v_mfma_f32_16x16x32_bf16 v[200:203], v[184:187], v[248:251], v[200:203]\n"
    " v_bitop3_b32 v3, v253, v254, s89 bitop3:0x7e\n"
    " v_mfma_f32_16x16x32_bf16 v[204:207], v[188:191], v[248:251], v[204:207]\n"
    " v_bitop3_b32 v2, v2, v3, s90 bitop3:0xa8\n"
    " v_cmp_ne_u32 vcc, 0, v2\n"
    " v_lshlrev_b32 v208, 16, v32\n"
    " v_and_b32 v209, s53, v32\n"
    " v_lshlrev_b32 v210, 16, v33\n"
    " v_and_b32 v211, s53, v33\n"
    " v_lshlrev_b32 v212, 16, v34\n"
    " v_and_b32 v213, s53, v34\n"
    " v_lshlrev_b32 v214, 16, v35\n"
    " v_and_b32 v215, s53, v35\n"
    " v_lshlrev_b32 v216, 16, v36\n"
    " v_and_b32 v217, s53, v36\n"
    " v_lshlrev_b32 v218, 16, v37\n"
    " v_and_b32 v219, s53, v37\n"
    " v_lshlrev_b32 v220, 16, v38\n"
    " v_and_b32 v221, s53, v38\n"
    " v_lshlrev_b32 v222, 16, v39\n"
    " v_and_b32 v223, s53, v39\n"
    " s_cbranch_vccnz .Lgs_94\n"
    ".Lgsb_95:\n"
    " v_mfma_f32_16x16x32_bf16 v[208:211], v[80:83], v[252:255], v[208:211]\n"
    " v_mfma_f32_16x16x32_bf16 v[212:215], v[84:87], v[252:255], v[212:215]\n"
    " s_waitcnt vmcnt(5)\n"
    " v_bitop3_b32 v2, v52, v55, s88 bitop3:0x7e\n"
    " v_mfma_f32_16x16x32_bf16 v[216:219], v[88:91], v[252:255], v[216:219]\n"
    " v_bitop3_b32 v3, v53, v54, s89 bitop3:0x7e\n"
    " v_mfma_f32_16x16x32_bf16 v[220:223], v[92:95], v[252:255], v[220:223]\n"
    " v_bitop3_b32 v2, v2, v3, s90 bitop3:0xa8\n"
    " v_cmp_ne_u32 vcc, 0, v2\n"
    " s_cbranch_vccnz .Lgs_97\n"
    ".Lgsb_98:\n"
    " v_mfma_f32_16x16x32_bf16 v[208:211], v[96:99], v[52:55], v[208:211]\n"
    " v_mfma_f32_16x16x32_bf16 v[212:215], v[100:103], v[52:55], v[212:215]\n"
    " s_waitcnt vmcnt(4)\n"
    " v_bitop3_b32 v2, v56, v59, s88 bitop3:0x7e\n"
    " v_mfma_f32_16x16x32_bf16 v[216:219], v[104:107], v[52:55], v[216:219]\n"
    " v_bitop3_b32 v3, v57, v58, s89 bitop3:0x7e\n"
    " v_mfma_f32_16x16x32_bf16 v[220:223], v[108:111], v[52:55], v[220:223]\n"
    " v_bitop3_b32 v2, v2, v3, s90 bitop3:0xa8\n"
    " v_cmp_ne_u32 vcc, 0, v2\n"
    " s_cbranch_vccnz .Lgs_100\n"
    ".Lgsb_101:\n"
    " v_mfma_f32_16x16x32_bf16 v[208:211], v[112:115], v[56:59], v[208:211]\n"
    " v_subrev_u32 v48, 66560, v48\n"
    " v_subrev_u32 v49, 66560, v49\n"
    " v_subrev_u32 v50, 66560, v50\n"
    " v_subrev_u32 v51, 66560, v51\n"
    " v_mfma_f32_16x16x32_bf16 v[212:215], v[116:119], v[56:59], v[212:215]\n"
    " s_waitcnt vmcnt(3)\n"
    " v_bitop3_b32 v2, v60, v63, s88 bitop3:0x7e\n"
    " v_mfma_f32_16x16x32_bf16 v[216:219], v[120:123], v[56:59], v[216:219]\n"
    " v_bitop3_b32 v3, v61, v62, s89 bitop3:0x7e\n"
    " v_mfma_f32_16x16x32_bf16 v[220:223], v[124:127], v[56:59], v[220:223]\n"
    " v_bitop3_b32 v2, v2, v3, s90 bitop3:0xa8\n"
    " v_cmp_ne_u32 vcc, 0, v2\n"
    " s_cbranch_vccnz .Lgs_103\n"
    ".Lgsb_104:\n"
    " v_mfma_f32_16x16x32_bf16 v[208:211], v[128:131], v[60:63], v[208:211]\n"
    " ds_write_b128 v48, v[192:195] offset:0\n"
    " v_mfma_f32_16x16x32_bf16 v[212:215], v[132:135], v[60:63], v[212:215]\n"
    " s_waitcnt vmcnt(2)\n"
    " v_bitop3_b32 v2, v64, v67, s88 bitop3:0x7e\n"
    " v_mfma_f32_16x16x32_bf16 v[216:219], v[136:139], v[60:63], v[216:219]\n"
    " v_bitop3_b32 v3, v65, v66, s89 bitop3:0x7e\n"
    " v_mfma_f32_16x16x32_bf16 v[220:223], v[140:143], v[60:63], v[220:223]\n"
    " v_bitop3_b32 v2, v2, v3, s90 bitop3:0xa8\n"
    " v_cmp_ne_u32 vcc, 0, v2\n"
    " s_cbranch_vccnz .Lgs_106\n"
    ".Lgsb_107:\n"
    " v_mfma_f32_16x16x32_bf16 v[208:211], v[144:147], v[64:67], v[208:211]\n"
    " ds_write_b128 v49, v[196:199] offset:1024\n"
    " v_mfma_f32_16x16x32_bf16 v[212:215], v[148:151], v[64:67], v[212:215]\n"
    " s_waitcnt vmcnt(1)\n"
    " v_bitop3_b32 v2, v68, v71, s88 bitop3:0x7e\n"
    " v_mfma_f32_16x16x32_bf16 v[216:219], v[152:155], v[64:67], v[216:219]\n"
    " v_bitop3_b32 v3, v69, v70, s89 bitop3:0x7e\n"
    " v_mfma_f32_16x16x32_bf16 v[220:223], v[156:159], v[64:67], v[220:223]\n"
    " v_bitop3_b32 v2, v2, v3, s90 bitop3:0xa8\n"
    " v_cmp_ne_u32 vcc, 0, v2\n"
    " s_cbranch_vccnz .Lgs_109\n"
    ".Lgsb_110:\n"
    " v_mfma_f32_16x16x32_bf16 v[208:211], v[160:163], v[68:71], v[208:211]\n"
    " ds_write_b128 v50, v[200:203] offset:2048\n"
    " v_mfma_f32_16x16x32_bf16 v[212:215], v[164:167], v[68:71], v[212:215]\n"
    " s_waitcnt vmcnt(0)\n"
    " v_bitop3_b32 v2, v72, v75, s88 bitop3:0x7e\n"
    " v_mfma_f32_16x16x32_bf16 v[216:219], v[168:171], v[68:71], v[216:219]\n"
    " v_bitop3_b32 v3, v73, v74, s89 bitop3:0x7e\n"
    " v_mfma_f32_16x16x32_bf16 v[220:223], v[172:175], v[68:71], v[220:223]\n"
    " v_bitop3_b32 v2, v2, v3, s90 bitop3:0xa8\n"
    " v_cmp_ne_u32 vcc, 0, v2\n"
    " s_cbranch_vccnz .Lgs_112\n"
    ".Lgsb_113:\n"
    " v_mfma_f32_16x16x32_bf16 v[208:211], v[176:179], v[72:75], v[208:211]\n"
    " ds_write_b128 v51, v[204:207] offset:3072\n"
    " v_mfma_f32_16x16x32_bf16 v[212:215], v[180:183], v[72:75], v[212:215]\n"
    " v_mfma_f32_16x16x32_bf16 v[216:219], v[184:187], v[72:75], v[216:219]\n"
    " v_mfma_f32_16x16x32_bf16 v[220:223], v[188:191], v[72:75], v[220:223]\n"
    " v_and_b32 v5, 7, v0\n"
    " v_bfe_u32 v6, v0, 3, 4\n"
    " v_lshlrev_b32 v7, 1, v5\n"
    " v_xor_b32 v6, v6, v7\n"
    " v_lshlrev_b32 v6, 4, v6\n"
    " v_lshl_add_u32 v6, v5, 9, v6\n"
    " v_bfe_u32 v7, v0, 7, 1\n"
    " v_lshl_add_u32 v6, v7, 12, v6\n"
    " ds_write_b128 v48, v[208:211] offset:4096\n"
    " ds_write_b128 v49, v[212:215] offset:5120\n"
    " ds_write_b128 v50, v[216:219] offset:6144\n"
    " ds_write_b128 v51, v[220:223] offset:7168\n"
    " s_cmp_lt_u32 s44, 4\n"
    " s_cselect_b32 s62, 66560, 0\n"
    " v_add_u32 v6, s62, v6\n"
    " s_lshl_b32 s60, s48, 16\n"
    " s_lshl_b32 s61, s49, 7\n"
    " s_add_u32 s60, s60, s61\n"
    " v_lshrrev_b32 v7, 3, v0\n"
    " v_lshlrev_b32 v5, 4, v5\n"
    " v_lshl_add_u32 v5, v7, 10, v5\n"
    " v_add_u32 v5, s60, v5\n"
    " s_waitcnt lgkmcnt(0)\n"
    " s_branch .Lb7\n"
    ".Lfl_1:\n"
    ".Lfp_3:\n"
    " buffer_load_dwordx2 v[16:17], v5, s[12:15], 0 offen sc1\n"
    " s_waitcnt vmcnt(0)\n"
    " v_xor_b32 v16, s50, v16\n"
    " v_xor_b32 v17, s51, v17\n"
    " v_or_b32 v16, v16, v17\n"
    " v_cmp_ne_u32 vcc, 0, v16\n"
    " s_cbranch_vccz .Lsp_4\n"
    " s_sub_u32 s57, s57, 1\n"
    " s_cbranch_scc0 .Lfp_3\n"
    " s_mov_b32 s57, 0\n"
    ".Lsp_4:\n"
    " s_branch .Lflb_2\n"
    ".Lkv_5:\n"
    " buffer_load_dwordx4 v[16:19], v4, s[12:15], s66 offen sc1\n"
    " buffer_load_dwordx4 v[20:23], v4, s[12:15], s67 offen sc1\n"
    " s_waitcnt vmcnt(1)\n"
    " ds_write_b128 v24, v[16:19]\n"
    " v_max3_u32 v5, v16, v17, v18\n"
    " s_waitcnt vmcnt(0)\n"
    " ds_write_b128 v24, v[20:23] offset:4608\n"
    " v_max3_u32 v5, v5, v19, v20\n"
    " v_max3_u32 v5, v5, v21, v22\n"
    " v_max_u32 v5, v5, v23\n"
    " v_cmp_eq_u32 vcc, -1, v5\n"
    " s_cbranch_vccz .Lsp_7\n"
    " s_sub_u32 s57, s57, 1\n"
    " s_cbranch_scc0 .Lkv_5\n"
    " s_mov_b32 s57, 0\n"
    ".Lsp_7:\n"
    " s_branch .Lkvb_6\n"
    ".Lh7:\n"
    " s_waitcnt vmcnt(0)\n"
    " s_branch .Lpre\n"
    ".Lhs_24:\n"
    " buffer_load_dwordx4 v[8:11], v1, s[12:15], s69 offen offset:0 sc1\n"
    " buffer_load_dwordx4 v[12:15], v1, s[12:15], s69 offen offset:1024 sc1\n"
    " buffer_load_dwordx4 v[16:19], v1, s[12:15], s69 offen offset:2048 sc1\n"
    " buffer_load_dwordx4 v[20:23], v1, s[12:15], s69 offen offset:3072 sc1\n"
    " buffer_load_dwordx4 v[24:27], v1, s[12:15], s66 offen offset:0 sc1\n"
    " buffer_load_dwordx4 v[28:31], v1, s[12:15], s66 offen offset:1024 sc1\n"
    " buffer_load_dwordx4 v[32:35], v1, s[12:15], s66 offen offset:2048 sc1\n"
    " buffer_load_dwordx4 v[36:39], v1, s[12:15], s66 offen offset:3072 sc1\n"
    " s_waitcnt vmcnt(7)\n"
    " ds_write_b128 v7, v[8:11] offset:0\n"
    " v_max3_u32 v5, v8, v9, v10\n"
    " v_max3_u32 v5, v10, v11, v5\n"
    " s_waitcnt vmcnt(6)\n"
    " ds_write_b128 v7, v[12:15] offset:1024\n"
    " v_max3_u32 v5, v12, v13, v5\n"
    " v_max3_u32 v5, v14, v15, v5\n"
    " s_waitcnt vmcnt(5)\n"
    " ds_write_b128 v7, v[16:19] offset:2048\n"
    " v_max3_u32 v5, v16, v17, v5\n"
    " v_max3_u32 v5, v18, v19, v5\n"
    " s_waitcnt vmcnt(4)\n"
    " ds_write_b128 v7, v[20:23] offset:3072\n"
    " v_max3_u32 v5, v20, v21, v5\n"
    " v_max3_u32 v5, v22, v23, v5\n"
    " s_waitcnt vmcnt(3)\n"
    " ds_write_b128 v7, v[24:27] offset:4096\n"
    " v_max3_u32 v5, v24, v25, v5\n"
    " v_max3_u32 v5, v26, v27, v5\n"
    " s_waitcnt vmcnt(2)\n"
    " ds_write_b128 v7, v[28:31] offset:5120\n"
    " v_max3_u32 v5, v28, v29, v5\n"
    " v_max3_u32 v5, v30, v31, v5\n"
    " s_waitcnt vmcnt(1)\n"
    " ds_write_b128 v7, v[32:35] offset:6144\n"
    " v_max3_u32 v5, v32, v33, v5\n"
    " v_max3_u32 v5, v34, v35, v5\n"
    " s_waitcnt vmcnt(0)\n"
    " ds_write_b128 v7, v[36:39] offset:7168\n"
    " v_max3_u32 v5, v36, v37, v5\n"
    " v_max3_u32 v5, v38, v39, v5\n"
    " v_cmp_eq_u32 vcc, -1, v5\n"
    " s_cbranch_vccz .Lsp_26\n"
    " s_sub_u32 s57, s57, 1\n"
    " s_cbranch_scc0 .Lhs_24\n"
    " s_mov_b32 s57, 0\n"
    ".Lsp_26:\n"
    " s_branch .Lhsb_25\n"
    ".Lfl_27:\n"
    ".Lfp_29:\n"
    " buffer_load_dwordx2 v[2:3], v6, s[12:15], 0 offen sc1\n"
    " s_waitcnt vmcnt(0)\n"
    " v_xor_b32 v2, s50, v2\n"
    " v_xor_b32 v3, s51, v3\n"
    " v_or_b32 v2, v2, v3\n"
    " v_cmp_ne_u32 vcc, 0, v2\n"
    " s_cbranch_vccz .Lsp_30\n"
    " s_sleep 4\n"
    " s_sub_u32 s57, s57, 1\n"
    " s_cbranch_scc0 .Lfp_29\n"
    " s_mov_b32 s57, 0\n"
    ".Lsp_30:\n"
    " s_branch .Lflb_28\n"
    ".Lgs_31:\n"
    " buffer_load_dwordx4 v[224:227], v1, s[12:15], s72 offen offset:0 sc1\n"
    " buffer_load_dwordx4 v[228:231], v1, s[12:15], s72 offen offset:1024 sc1\n"
    " buffer_load_dwordx4 v[232:235], v1, s[12:15], s72 offen offset:2048 sc1\n"
    " buffer_load_dwordx4 v[236:239], v1, s[12:15], s72 offen offset:3072 sc1\n"
    " buffer_load_dwordx4 v[240:243], v1, s[12:15], s73 offen offset:0 sc1\n"
    " buffer_load_dwordx4 v[244:247], v1, s[12:15], s73 offen offset:1024 sc1\n"
    " buffer_load_dwordx4 v[248:251], v1, s[12:15], s73 offen offset:2048 sc1\n"
    " s_waitcnt vmcnt(0)\n"
    " v_bitop3_b32 v2, v224, v227, s88 bitop3:0x7e\n"
    " v_bitop3_b32 v3, v225, v226, s89 bitop3:0x7e\n"
    " v_bitop3_b32 v2, v2, v3, s90 bitop3:0xa8\n"
    " v_cmp_ne_u32 vcc, 0, v2\n"
    " s_cbranch_vccz .Lsp_33\n"
    " s_sleep 1\n"
    " s_sub_u32 s57, s57, 1\n"
    " s_cbranch_scc0 .Lgs_31\n"
    " s_mov_b32 s57, 0\n"
    ".Lsp_33:\n"
    " s_branch .Lgsb_32\n"
    ".Lgs_34:\n"
    " buffer_load_dwordx4 v[228:231], v1, s[12:15], s72 offen offset:1024 sc1\n"
    " buffer_load_dwordx4 v[232:235], v1, s[12:15], s72 offen offset:2048 sc1\n"
    " buffer_load_dwordx4 v[236:239], v1, s[12:15], s72 offen offset:3072 sc1\n"
    " buffer_load_dwordx4 v[240:243], v1, s[12:15], s73 offen offset:0 sc1\n"
    " buffer_load_dwordx4 v[244:247], v1, s[12:15], s73 offen offset:1024 sc1\n"
    " buffer_load_dwordx4 v[248:251], v1, s[12:15], s73 offen offset:2048 sc1\n"
    " buffer_load_dwordx4 v[252:255], v1, s[12:15], s74 offen offset:0 sc1\n"
    " s_waitcnt vmcnt(0)\n"
    " v_bitop3_b32 v2, v228, v231, s88 bitop3:0x7e\n"
    " v_bitop3_b32 v3, v229, v230, s89 bitop3:0x7e\n"
    " v_bitop3_b32 v2, v2, v3, s90 bitop3:0xa8\n"
    " v_cmp_ne_u32 vcc, 0, v2\n"
    " s_cbranch_vccz .Lsp_36\n"
    " s_sleep 1\n"
    " s_sub_u32 s57, s57, 1\n"
    " s_cbranch_scc0 .Lgs_34\n"
    " s_mov_b32 s57, 0\n"
    ".Lsp_36:\n"
    " s_branch .Lgsb_35\n"
    ".Lgs_37:\n"
    " buffer_load_dwordx4 v[232:235], v1, s[12:15], s72 offen offset:2048 sc1\n"
    " buffer_load_dwordx4 v[236:239], v1, s[12:15], s72 offen offset:3072 sc1\n"
    " buffer_load_dwordx4 v[240:243], v1, s[12:15], s73 offen offset:0 sc1\n"
    " buffer_load_dwordx4 v[244:247], v1, s[12:15], s73 offen offset:1024 sc1\n"
    " buffer_load_dwordx4 v[248:251], v1, s[12:15], s73 offen offset:2048 sc1\n"
    " buffer_load_dwordx4 v[252:255], v1, s[12:15], s74 offen offset:0 sc1\n"
    " buffer_load_dwordx4 v[52:55], v1, s[12:15], s74 offen offset:1024 sc1\n"
    " s_waitcnt vmcnt(0)\n"
    " v_bitop3_b32 v2, v232, v235, s88 bitop3:0x7e\n"
    " v_bitop3_b32 v3, v233, v234, s89 bitop3:0x7e\n"
    " v_bitop3_b32 v2, v2, v3, s90 bitop3:0xa8\n"
    " v_cmp_ne_u32 vcc, 0, v2\n"
    " s_cbranch_vccz .Lsp_39\n"
    " s_sleep 1\n"
    " s_sub_u32 s57, s57, 1\n"
    " s_cbranch_scc0 .Lgs_37\n"
    " s_mov_b32 s57, 0\n"
    ".Lsp_39:\n"
    " s_branch .Lgsb_38\n"
    ".Lgs_40:\n"
    " buffer_load_dwordx4 v[236:239], v1, s[12:15], s72 offen offset:3072 sc1\n"
    " buffer_load_dwordx4 v[240:243], v1, s[12:15], s73 offen offset:0 sc1\n"
    " buffer_load_dwordx4 v[244:247], v1, s[12:15], s73 offen offset:1024 sc1\n"
    " buffer_load_dwordx4 v[248:251], v1, s[12:15], s73 offen offset:2048 sc1\n"
    " buffer_load_dwordx4 v[252:255], v1, s[12:15], s74 offen offset:0 sc1\n"
    " buffer_load_dwordx4 v[52:55], v1, s[12:15], s74 offen offset:1024 sc1\n"
    " buffer_load_dwordx4 v[56:59], v1, s[12:15], s74 offen offset:2048 sc1\n"
    " s_waitcnt vmcnt(0)\n"
    " v_bitop3_b32 v2, v236, v239, s88 bitop3:0x7e\n"
    " v_bitop3_b32 v3, v237, v238, s89 bitop3:0x7e\n"
    " v_bitop3_b32 v2, v2, v3, s90 bitop3:0xa8\n"
    " v_cmp_ne_u32 vcc, 0, v2\n"
    " s_cbranch_vccz .Lsp_42\n"
    " s_sleep 1\n"
    " s_sub_u32 s57, s57, 1\n"
    " s_cbranch_scc0 .Lgs_40\n"
    " s_mov_b32 s57, 0\n"
    ".Lsp_42:\n"
    " s_branch .Lgsb_41\n"
    ".Lgs_43:\n"
    " buffer_load_dwordx4 v[240:243], v1, s[12:15], s73 offen offset:0 sc1\n"
    " buffer_load_dwordx4 v[244:247], v1, s[12:15], s73 offen offset:1024 sc1\n"
    " buffer_load_dwordx4 v[248:251], v1, s[12:15], s73 offen offset:2048 sc1\n"
    " buffer_load_dwordx4 v[252:255], v1, s[12:15], s74 offen offset:0 sc1\n"
    " buffer_load_dwordx4 v[52:55], v1, s[12:15], s74 offen offset:1024 sc1\n"
    " buffer_load_dwordx4 v[56:59], v1, s[12:15], s74 offen offset:2048 sc1\n"
    " buffer_load_dwordx4 v[60:63], v1, s[12:15], s74 offen offset:3072 sc1\n"
    " s_waitcnt vmcnt(0)\n"
    " v_bitop3_b32 v2, v240, v243, s88 bitop3:0x7e\n"
    " v_bitop3_b32 v3, v241, v242, s89 bitop3:0x7e\n"
    " v_bitop3_b32 v2, v2, v3, s90 bitop3:0xa8\n"
    " v_cmp_ne_u32 vcc, 0, v2\n"
    " s_cbranch_vccz .Lsp_45\n"
    " s_sleep 1\n"
    " s_sub_u32 s57, s57, 1\n"
    " s_cbranch_scc0 .Lgs_43\n"
    " s_mov_b32 s57, 0\n"
    ".Lsp_45:\n"
    " s_branch .Lgsb_44\n"
    ".Lgs_46:\n"
    " buffer_load_dwordx4 v[244:247], v1, s[12:15], s73 offen offset:1024 sc1\n"
    " buffer_load_dwordx4 v[248:251], v1, s[12:15], s73 offen offset:2048 sc1\n"
    " buffer_load_dwordx4 v[252:255], v1, s[12:15], s74 offen offset:0 sc1\n"
    " buffer_load_dwordx4 v[52:55], v1, s[12:15], s74 offen offset:1024 sc1\n"
    " buffer_load_dwordx4 v[56:59], v1, s[12:15], s74 offen offset:2048 sc1\n"
    " buffer_load_dwordx4 v[60:63], v1, s[12:15], s74 offen offset:3072 sc1\n"
    " buffer_load_dwordx4 v[64:67], v1, s[12:15], s75 offen offset:0 sc1\n"
    " s_waitcnt vmcnt(0)\n"
    " v_bitop3_b32 v2, v244, v247, s88 bitop3:0x7e\n"
    " v_bitop3_b32 v3, v245, v246, s89 bitop3:0x7e\n"
    " v_bitop3_b32 v2, v2, v3, s90 bitop3:0xa8\n"
    " v_cmp_ne_u32 vcc, 0, v2\n"
    " s_cbranch_vccz .Lsp_48\n"
    " s_sleep 1\n"
    " s_sub_u32 s57, s57, 1\n"
    " s_cbranch_scc0 .Lgs_46\n"
    " s_mov_b32 s57, 0\n"
    ".Lsp_48:\n"
    " s_branch .Lgsb_47\n"
    ".Lgs_49:\n"
    " buffer_load_dwordx4 v[248:251], v1, s[12:15], s73 offen offset:2048 sc1\n"
    " buffer_load_dwordx4 v[252:255], v1, s[12:15], s74 offen offset:0 sc1\n"
    " buffer_load_dwordx4 v[52:55], v1, s[12:15], s74 offen offset:1024 sc1\n"
    " buffer_load_dwordx4 v[56:59], v1, s[12:15], s74 offen offset:2048 sc1\n"
    " buffer_load_dwordx4 v[60:63], v1, s[12:15], s74 offen offset:3072 sc1\n"
    " buffer_load_dwordx4 v[64:67], v1, s[12:15], s75 offen offset:0 sc1\n"
    " buffer_load_dwordx4 v[68:71], v1, s[12:15], s75 offen offset:1024 sc1\n"
    " s_waitcnt vmcnt(0)\n"
    " v_bitop3_b32 v2, v248, v251, s88 bitop3:0x7e\n"
    " v_bitop3_b32 v3, v249, v250, s89 bitop3:0x7e\n"
    " v_bitop3_b32 v2, v2, v3, s90 bitop3:0xa8\n"
    " v_cmp_ne_u32 vcc, 0, v2\n"
    " s_cbranch_vccz .Lsp_51\n"
    " s_sleep 1\n"
    " s_sub_u32 s57, s57, 1\n"
    " s_cbranch_scc0 .Lgs_49\n"
    " s_mov_b32 s57, 0\n"
    ".Lsp_51:\n"
    " s_branch .Lgsb_50\n"
    ".Lgs_52:\n"
    " buffer_load_dwordx4 v[252:255], v1, s[12:15], s74 offen offset:0 sc1\n"
    " buffer_load_dwordx4 v[52:55], v1, s[12:15], s74 offen offset:1024 sc1\n"
    " buffer_load_dwordx4 v[56:59], v1, s[12:15], s74 offen offset:2048 sc1\n"
    " buffer_load_dwordx4 v[60:63], v1, s[12:15], s74 offen offset:3072 sc1\n"
    " buffer_load_dwordx4 v[64:67], v1, s[12:15], s75 offen offset:0 sc1\n"
    " buffer_load_dwordx4 v[68:71], v1, s[12:15], s75 offen offset:1024 sc1\n"
    " buffer_load_dwordx4 v[72:75], v1, s[12:15], s75 offen offset:2048 sc1\n"
    " s_waitcnt vmcnt(0)\n"
    " v_bitop3_b32 v2, v252, v255, s88 bitop3:0x7e\n"
    " v_bitop3_b32 v3, v253, v254, s89 bitop3:0x7e\n"
    " v_bitop3_b32 v2, v2, v3, s90 bitop3:0xa8\n"
    " v_cmp_ne_u32 vcc, 0, v2\n"
    " s_cbranch_vccz .Lsp_54\n"
    " s_sleep 1\n"
    " s_sub_u32 s57, s57, 1\n"
    " s_cbranch_scc0 .Lgs_52\n"
    " s_mov_b32 s57, 0\n"
    ".Lsp_54:\n"
    " s_branch .Lgsb_53\n"
    ".Lgs_55:\n"
    " buffer_load_dwordx4 v[52:55], v1, s[12:15], s74 offen offset:1024 sc1\n"
    " buffer_load_dwordx4 v[56:59], v1, s[12:15], s74 offen offset:2048 sc1\n"
    " buffer_load_dwordx4 v[60:63], v1, s[12:15], s74 offen offset:3072 sc1\n"
    " buffer_load_dwordx4 v[64:67], v1, s[12:15], s75 offen offset:0 sc1\n"
    " buffer_load_dwordx4 v[68:71], v1, s[12:15], s75 offen offset:1024 sc1\n"
    " buffer_load_dwordx4 v[72:75], v1, s[12:15], s75 offen offset:2048 sc1\n"
    " buffer_load_dwordx4 v[224:227], v1, s[12:15], s76 offen offset:0 sc1\n"
    " s_waitcnt vmcnt(0)\n"
    " v_bitop3_b32 v2, v52, v55, s88 bitop3:0x7e\n"
    " v_bitop3_b32 v3, v53, v54, s89 bitop3:0x7e\n"
    " v_bitop3_b32 v2, v2, v3, s90 bitop3:0xa8\n"
    " v_cmp_ne_u32 vcc, 0, v2\n"
    " s_cbranch_vccz .Lsp_57\n"
    " s_sleep 1\n"
    " s_sub_u32 s57, s57, 1\n"
    " s_cbranch_scc0 .Lgs_55\n"
    " s_mov_b32 s57, 0\n"
    ".Lsp_57:\n"
    " s_branch .Lgsb_56\n"
    ".Lgs_58:\n"
    " buffer_load_dwordx4 v[56:59], v1, s[12:15], s74 offen offset:2048 sc1\n"
    " buffer_load_dwordx4 v[60:63], v1, s[12:15], s74 offen offset:3072 sc1\n"
    " buffer_load_dwordx4 v[64:67], v1, s[12:15], s75 offen offset:0 sc1\n"
    " buffer_load_dwordx4 v[68:71], v1, s[12:15], s75 offen offset:1024 sc1\n"
    " buffer_load_dwordx4 v[72:75], v1, s[12:15], s75 offen offset:2048 sc1\n"
    " buffer_load_dwordx4 v[224:227], v1, s[12:15], s76 offen offset:0 sc1\n"
    " buffer_load_dwordx4 v[228:231], v1, s[12:15], s76 offen offset:1024 sc1\n"
    " s_waitcnt vmcnt(0)\n"
    " v_bitop3_b32 v2, v56, v59, s88 bitop3:0x7e\n"
    " v_bitop3_b32 v3, v57, v58, s89 bitop3:0x7e\n"
    " v_bitop3_b32 v2, v2, v3, s90 bitop3:0xa8\n"
    " v_cmp_ne_u32 vcc, 0, v2\n"
    " s_cbranch_vccz .Lsp_60\n"
    " s_sleep 1\n"
    " s_sub_u32 s57, s57, 1\n"
    " s_cbranch_scc0 .Lgs_58\n"
    " s_mov_b32 s57, 0\n"
    ".Lsp_60:\n"
    " s_branch .Lgsb_59\n"
    ".Lgs_61:\n"
    " buffer_load_dwordx4 v[60:63], v1, s[12:15], s74 offen offset:3072 sc1\n"
    " buffer_load_dwordx4 v[64:67], v1, s[12:15], s75 offen offset:0 sc1\n"
    " buffer_load_dwordx4 v[68:71], v1, s[12:15], s75 offen offset:1024 sc1\n"
    " buffer_load_dwordx4 v[72:75], v1, s[12:15], s75 offen offset:2048 sc1\n"
    " buffer_load_dwordx4 v[224:227], v1, s[12:15], s76 offen offset:0 sc1\n"
    " buffer_load_dwordx4 v[228:231], v1, s[12:15], s76 offen offset:1024 sc1\n"
    " buffer_load_dwordx4 v[232:235], v1, s[12:15], s76 offen offset:2048 sc1\n"
    " s_waitcnt vmcnt(0)\n"
    " v_bitop3_b32 v2, v60, v63, s88 bitop3:0x7e\n"
    " v_bitop3_b32 v3, v61, v62, s89 bitop3:0x7e\n"
    " v_bitop3_b32 v2, v2, v3, s90 bitop3:0xa8\n"
    " v_cmp_ne_u32 vcc, 0, v2\n"
    " s_cbranch_vccz .Lsp_63\n"
    " s_sleep 1\n"
    " s_sub_u32 s57, s57, 1\n"
    " s_cbranch_scc0 .Lgs_61\n"
    " s_mov_b32 s57, 0\n"
    ".Lsp_63:\n"
    " s_branch .Lgsb_62\n"
    ".Lgs_64:\n"
    " buffer_load_dwordx4 v[64:67], v1, s[12:15], s75 offen offset:0 sc1\n"
    " buffer_load_dwordx4 v[68:71], v1, s[12:15], s75 offen offset:1024 sc1\n"
    " buffer_load_dwordx4 v[72:75], v1, s[12:15], s75 offen offset:2048 sc1\n"
    " buffer_load_dwordx4 v[224:227], v1, s[12:15], s76 offen offset:0 sc1\n"
    " buffer_load_dwordx4 v[228:231], v1, s[12:15], s76 offen offset:1024 sc1\n"
    " buffer_load_dwordx4 v[232:235], v1, s[12:15], s76 offen offset:2048 sc1\n"
    " buffer_load_dwordx4 v[236:239], v1, s[12:15], s76 offen offset:3072 sc1\n"
    " s_waitcnt vmcnt(0)\n"
    " v_bitop3_b32 v2, v64, v67, s88 bitop3:0x7e\n"
    " v_bitop3_b32 v3, v65, v66, s89 bitop3:0x7e\n"
    " v_bitop3_b32 v2, v2, v3, s90 bitop3:0xa8\n"
    " v_cmp_ne_u32 vcc, 0, v2\n"
    " s_cbranch_vccz .Lsp_66\n"
    " s_sleep 1\n"
    " s_sub_u32 s57, s57, 1\n"
    " s_cbranch_scc0 .Lgs_64\n"
    " s_mov_b32 s57, 0\n"
    ".Lsp_66:\n"
    " s_branch .Lgsb_65\n"
    ".Lgs_67:\n"
    " buffer_load_dwordx4 v[68:71], v1, s[12:15], s75 offen offset:1024 sc1\n"
    " buffer_load_dwordx4 v[72:75], v1, s[12:15], s75 offen offset:2048 sc1\n"
    " buffer_load_dwordx4 v[224:227], v1, s[12:15], s76 offen offset:0 sc1\n"
    " buffer_load_dwordx4 v[228:231], v1, s[12:15], s76 offen offset:1024 sc1\n"
    " buffer_load_dwordx4 v[232:235], v1, s[12:15], s76 offen offset:2048 sc1\n"
    " buffer_load_dwordx4 v[236:239], v1, s[12:15], s76 offen offset:3072 sc1\n"
    " buffer_load_dwordx4 v[240:243], v1, s[12:15], s77 offen offset:0 sc1\n"
    " s_waitcnt vmcnt(0)\n"
    " v_bitop3_b32 v2, v68, v71, s88 bitop3:0x7e\n"
    " v_bitop3_b32 v3, v69, v70, s89 bitop3:0x7e\n"
    " v_bitop3_b32 v2, v2, v3, s90 bitop3:0xa8\n"
    " v_cmp_ne_u32 vcc, 0, v2\n"
    " s_cbranch_vccz .Lsp_69\n"
    " s_sleep 1\n"
    " s_sub_u32 s57, s57, 1\n"
    " s_cbranch_scc0 .Lgs_67\n"
    " s_mov_b32 s57, 0\n"
    ".Lsp_69:\n"
    " s_branch .Lgsb_68\n"
    ".Lgs_70:\n"
    " buffer_load_dwordx4 v[72:75], v1, s[12:15], s75 offen offset:2048 sc1\n"
    " buffer_load_dwordx4 v[224:227], v1, s[12:15], s76 offen offset:0 sc1\n"
    " buffer_load_dwordx4 v[228:231], v1, s[12:15], s76 offen offset:1024 sc1\n"
    " buffer_load_dwordx4 v[232:235], v1, s[12:15], s76 offen offset:2048 sc1\n"
    " buffer_load_dwordx4 v[236:239], v1, s[12:15], s76 offen offset:3072 sc1\n"
    " buffer_load_dwordx4 v[240:243], v1, s[12:15], s77 offen offset:0 sc1\n"
    " buffer_load_dwordx4 v[244:247], v1, s[12:15], s77 offen offset:1024 sc1\n"
    " s_waitcnt vmcnt(0)\n"
    " v_bitop3_b32 v2, v72, v75, s88 bitop3:0x7e\n"
    " v_bitop3_b32 v3, v73, v74, s89 bitop3:0x7e\n"
    " v_bitop3_b32 v2, v2, v3, s90 bitop3:0xa8\n"
    " v_cmp_ne_u32 vcc, 0, v2\n"
    " s_cbranch_vccz .Lsp_72\n"
    " s_sleep 1\n"
    " s_sub_u32 s57, s57, 1\n"
    " s_cbranch_scc0 .Lgs_70\n"
    " s_mov_b32 s57, 0\n"
    ".Lsp_72:\n"
    " s_branch .Lgsb_71\n"
    ".Lgs_73:\n"
    " buffer_load_dwordx4 v[224:227], v1, s[12:15], s76 offen offset:0 sc1\n"
    " buffer_load_dwordx4 v[228:231], v1, s[12:15], s76 offen offset:1024 sc1\n"
    " buffer_load_dwordx4 v[232:235], v1, s[12:15], s76 offen offset:2048 sc1\n"
    " buffer_load_dwordx4 v[236:239], v1, s[12:15], s76 offen offset:3072 sc1\n"
    " buffer_load_dwordx4 v[240:243], v1, s[12:15], s77 offen offset:0 sc1\n"
    " buffer_load_dwordx4 v[244:247], v1, s[12:15], s77 offen offset:1024 sc1\n"
    " buffer_load_dwordx4 v[248:251], v1, s[12:15], s77 offen offset:2048 sc1\n"
    " s_waitcnt vmcnt(0)\n"
    " v_bitop3_b32 v2, v224, v227, s88 bitop3:0x7e\n"
    " v_bitop3_b32 v3, v225, v226, s89 bitop3:0x7e\n"
    " v_bitop3_b32 v2, v2, v3, s90 bitop3:0xa8\n"
    " v_cmp_ne_u32 vcc, 0, v2\n"
    " s_cbranch_vccz .Lsp_75\n"
    " s_sleep 1\n"
    " s_sub_u32 s57, s57, 1\n"
    " s_cbranch_scc0 .Lgs_73\n"
    " s_mov_b32 s57, 0\n"
    ".Lsp_75:\n"
    " s_branch .Lgsb_74\n"
    ".Lgs_76:\n"
    " buffer_load_dwordx4 v[228:231], v1, s[12:15], s76 offen offset:1024 sc1\n"
    " buffer_load_dwordx4 v[232:235], v1, s[12:15], s76 offen offset:2048 sc1\n"
    " buffer_load_dwordx4 v[236:239], v1, s[12:15], s76 offen offset:3072 sc1\n"
    " buffer_load_dwordx4 v[240:243], v1, s[12:15], s77 offen offset:0 sc1\n"
    " buffer_load_dwordx4 v[244:247], v1, s[12:15], s77 offen offset:1024 sc1\n"
    " buffer_load_dwordx4 v[248:251], v1, s[12:15], s77 offen offset:2048 sc1\n"
    " buffer_load_dwordx4 v[252:255], v1, s[12:15], s78 offen offset:0 sc1\n"
    " s_waitcnt vmcnt(0)\n"
    " v_bitop3_b32 v2, v228, v231, s88 bitop3:0x7e\n"
    " v_bitop3_b32 v3, v229, v230, s89 bitop3:0x7e\n"
    " v_bitop3_b32 v2, v2, v3, s90 bitop3:0xa8\n"
    " v_cmp_ne_u32 vcc, 0, v2\n"
    " s_cbranch_vccz .Lsp_78\n"
    " s_sleep 1\n"
    " s_sub_u32 s57, s57, 1\n"
    " s_cbranch_scc0 .Lgs_76\n"
    " s_mov_b32 s57, 0\n"
    ".Lsp_78:\n"
    " s_branch .Lgsb_77\n"
    ".Lgs_79:\n"
    " buffer_load_dwordx4 v[232:235], v1, s[12:15], s76 offen offset:2048 sc1\n"
    " buffer_load_dwordx4 v[236:239], v1, s[12:15], s76 offen offset:3072 sc1\n"
    " buffer_load_dwordx4 v[240:243], v1, s[12:15], s77 offen offset:0 sc1\n"
    " buffer_load_dwordx4 v[244:247], v1, s[12:15], s77 offen offset:1024 sc1\n"
    " buffer_load_dwordx4 v[248:251], v1, s[12:15], s77 offen offset:2048 sc1\n"
    " buffer_load_dwordx4 v[252:255], v1, s[12:15], s78 offen offset:0 sc1\n"
    " buffer_load_dwordx4 v[52:55], v1, s[12:15], s78 offen offset:1024 sc1\n"
    " s_waitcnt vmcnt(0)\n"
    " v_bitop3_b32 v2, v232, v235, s88 bitop3:0x7e\n"
    " v_bitop3_b32 v3, v233, v234, s89 bitop3:0x7e\n"
    " v_bitop3_b32 v2, v2, v3, s90 bitop3:0xa8\n"
    " v_cmp_ne_u32 vcc, 0, v2\n"
    " s_cbranch_vccz .Lsp_81\n"
    " s_sleep 1\n"
    " s_sub_u32 s57, s57, 1\n"
    " s_cbranch_scc0 .Lgs_79\n"
    " s_mov_b32 s57, 0\n"
    ".Lsp_81:\n"
    " s_branch .Lgsb_80\n"
    ".Lgs_82:\n"
    " buffer_load_dwordx4 v[236:239], v1, s[12:15], s76 offen offset:3072 sc1\n"
    " buffer_load_dwordx4 v[240:243], v1, s[12:15], s77 offen offset:0 sc1\n"
    " buffer_load_dwordx4 v[244:247], v1, s[12:15], s77 offen offset:1024 sc1\n"
    " buffer_load_dwordx4 v[248:251], v1, s[12:15], s77 offen offset:2048 sc1\n"
    " buffer_load_dwordx4 v[252:255], v1, s[12:15], s78 offen offset:0 sc1\n"
    " buffer_load_dwordx4 v[52:55], v1, s[12:15], s78 offen offset:1024 sc1\n"
    " buffer_load_dwordx4 v[56:59], v1, s[12:15], s78 offen offset:2048 sc1\n"
    " s_waitcnt vmcnt(0)\n"
    " v_bitop3_b32 v2, v236, v239, s88 bitop3:0x7e\n"
    " v_bitop3_b32 v3, v237, v238, s89 bitop3:0x7e\n"
    " v_bitop3_b32 v2, v2, v3, s90 bitop3:0xa8\n"
    " v_cmp_ne_u32 vcc, 0, v2\n"
    " s_cbranch_vccz .Lsp_84\n"
    " s_sleep 1\n"
    " s_sub_u32 s57, s57, 1\n"
    " s_cbranch_scc0 .Lgs_82\n"
    " s_mov_b32 s57, 0\n"
    ".Lsp_84:\n"
    " s_branch .Lgsb_83\n"
    ".Lgs_85:\n"
    " buffer_load_dwordx4 v[240:243], v1, s[12:15], s77 offen offset:0 sc1\n"
    " buffer_load_dwordx4 v[244:247], v1, s[12:15], s77 offen offset:1024 sc1\n"
    " buffer_load_dwordx4 v[248:251], v1, s[12:15], s77 offen offset:2048 sc1\n"
    " buffer_load_dwordx4 v[252:255], v1, s[12:15], s78 offen offset:0 sc1\n"
    " buffer_load_dwordx4 v[52:55], v1, s[12:15], s78 offen offset:1024 sc1\n"
    " buffer_load_dwordx4 v[56:59], v1, s[12:15], s78 offen offset:2048 sc1\n"
    " buffer_load_dwordx4 v[60:63], v1, s[12:15], s78 offen offset:3072 sc1\n"
    " s_waitcnt vmcnt(0)\n"
    " v_bitop3_b32 v2, v240, v243, s88 bitop3:0x7e\n"
    " v_bitop3_b32 v3, v241, v242, s89 bitop3:0x7e\n"
    " v_bitop3_b32 v2, v2, v3, s90 bitop3:0xa8\n"
    " v_cmp_ne_u32 vcc, 0, v2\n"
    " s_cbranch_vccz .Lsp_87\n"
    " s_sleep 1\n"
    " s_sub_u32 s57, s57, 1\n"
    " s_cbranch_scc0 .Lgs_85\n"
    " s_mov_b32 s57, 0\n"
    ".Lsp_87:\n"
    " s_branch .Lgsb_86\n"
    ".Lgs_88:\n"
    " buffer_load_dwordx4 v[244:247], v1, s[12:15], s77 offen offset:1024 sc1\n"
    " buffer_load_dwordx4 v[248:251], v1, s[12:15], s77 offen offset:2048 sc1\n"
    " buffer_load_dwordx4 v[252:255], v1, s[12:15], s78 offen offset:0 sc1\n"
    " buffer_load_dwordx4 v[52:55], v1, s[12:15], s78 offen offset:1024 sc1\n"
    " buffer_load_dwordx4 v[56:59], v1, s[12:15], s78 offen offset:2048 sc1\n"
    " buffer_load_dwordx4 v[60:63], v1, s[12:15], s78 offen offset:3072 sc1\n"
    " buffer_load_dwordx4 v[64:67], v1, s[12:15], s79 offen offset:0 sc1\n"
    " s_waitcnt vmcnt(0)\n"
    " v_bitop3_b32 v2, v244, v247, s88 bitop3:0x7e\n"
    " v_bitop3_b32 v3, v245, v246, s89 bitop3:0x7e\n"
    " v_bitop3_b32 v2, v2, v3, s90 bitop3:0xa8\n"
    " v_cmp_ne_u32 vcc, 0, v2\n"
    " s_cbranch_vccz .Lsp_90\n"
    " s_sleep 1\n"
    " s_sub_u32 s57, s57, 1\n"
    " s_cbranch_scc0 .Lgs_88\n"
    " s_mov_b32 s57, 0\n"
    ".Lsp_90:\n"
    " s_branch .Lgsb_89\n"
    ".Lgs_91:\n"
    " buffer_load_dwordx4 v[248:251], v1, s[12:15], s77 offen offset:2048 sc1\n"
    " buffer_load_dwordx4 v[252:255], v1, s[12:15], s78 offen offset:0 sc1\n"
    " buffer_load_dwordx4 v[52:55], v1, s[12:15], s78 offen offset:1024 sc1\n"
    " buffer_load_dwordx4 v[56:59], v1, s[12:15], s78 offen offset:2048 sc1\n"
    " buffer_load_dwordx4 v[60:63], v1, s[12:15], s78 offen offset:3072 sc1\n"
    " buffer_load_dwordx4 v[64:67], v1, s[12:15], s79 offen offset:0 sc1\n"
    " buffer_load_dwordx4 v[68:71], v1, s[12:15], s79 offen offset:1024 sc1\n"
    " s_waitcnt vmcnt(0)\n"
    " v_bitop3_b32 v2, v248, v251, s88 bitop3:0x7e\n"
    " v_bitop3_b32 v3, v249, v250, s89 bitop3:0x7e\n"
    " v_bitop3_b32 v2, v2, v3, s90 bitop3:0xa8\n"
    " v_cmp_ne_u32 vcc, 0, v2\n"
    " s_cbranch_vccz .Lsp_93\n"
    " s_sleep 1\n"
    " s_sub_u32 s57, s57, 1\n"
    " s_cbranch_scc0 .Lgs_91\n"
    " s_mov_b32 s57, 0\n"
    ".Lsp_93:\n"
    " s_branch .Lgsb_92\n"
    ".Lgs_94:\n"
    " buffer_load_dwordx4 v[252:255], v1, s[12:15], s78 offen offset:0 sc1\n"
    " buffer_load_dwordx4 v[52:55], v1, s[12:15], s78 offen offset:1024 sc1\n"
    " buffer_load_dwordx4 v[56:59], v1, s[12:15], s78 offen offset:2048 sc1\n"
    " buffer_load_dwordx4 v[60:63], v1, s[12:15], s78 offen offset:3072 sc1\n"
    " buffer_load_dwordx4 v[64:67], v1, s[12:15], s79 offen offset:0 sc1\n"
    " buffer_load_dwordx4 v[68:71], v1, s[12:15], s79 offen offset:1024 sc1\n"
    " buffer_load_dwordx4 v[72:75], v1, s[12:15], s79 offen offset:2048 sc1\n"
    " s_waitcnt vmcnt(0)\n"
    " v_bitop3_b32 v2, v252, v255, s88 bitop3:0x7e\n"
    " v_bitop3_b32 v3, v253, v254, s89 bitop3:0x7e\n"
    " v_bitop3_b32 v2, v2, v3, s90 bitop3:0xa8\n"
    " v_cmp_ne_u32 vcc, 0, v2\n"
    " s_cbranch_vccz .Lsp_96\n"
    " s_sleep 1\n"
    " s_sub_u32 s57, s57, 1\n"
    " s_cbranch_scc0 .Lgs_94\n"
    " s_mov_b32 s57, 0\n"
    ".Lsp_96:\n"
    " s_branch .Lgsb_95\n"
    ".Lgs_97:\n"
    " buffer_load_dwordx4 v[52:55], v1, s[12:15], s78 offen offset:1024 sc1\n"
    " buffer_load_dwordx4 v[56:59], v1, s[12:15], s78 offen offset:2048 sc1\n"
    " buffer_load_dwordx4 v[60:63], v1, s[12:15], s78 offen offset:3072 sc1\n"
    " buffer_load_dwordx4 v[64:67], v1, s[12:15], s79 offen offset:0 sc1\n"
    " buffer_load_dwordx4 v[68:71], v1, s[12:15], s79 offen offset:1024 sc1\n"
    " buffer_load_dwordx4 v[72:75], v1, s[12:15], s79 offen offset:2048 sc1\n"
    " s_waitcnt vmcnt(0)\n"
    " v_bitop3_b32 v2, v52, v55, s88 bitop3:0x7e\n"
    " v_bitop3_b32 v3, v53, v54, s89 bitop3:0x7e\n"
    " v_bitop3_b32 v2, v2, v3, s90 bitop3:0xa8\n"
    " v_cmp_ne_u32 vcc, 0, v2\n"
    " s_cbranch_vccz .Lsp_99\n"
    " s_sleep 1\n"
    " s_sub_u32 s57, s57, 1\n"
    " s_cbranch_scc0 .Lgs_97\n"
    " s_mov_b32 s57, 0\n"
    ".Lsp_99:\n"
    " s_branch .Lgsb_98\n"
    ".Lgs_100:\n"
    " buffer_load_dwordx4 v[56:59], v1, s[12:15], s78 offen offset:2048 sc1\n"
    " buffer_load_dwordx4 v[60:63], v1, s[12:15], s78 offen offset:3072 sc1\n"
    " buffer_load_dwordx4 v[64:67], v1, s[12:15], s79 offen offset:0 sc1\n"
    " buffer_load_dwordx4 v[68:71], v1, s[12:15], s79 offen offset:1024 sc1\n"
    " buffer_load_dwordx4 v[72:75], v1, s[12:15], s79 offen offset:2048 sc1\n"
    " s_waitcnt vmcnt(0)\n"
    " v_bitop3_b32 v2, v56, v59, s88 bitop3:0x7e\n"
    " v_bitop3_b32 v3, v57, v58, s89 bitop3:0x7e\n"
    " v_bitop3_b32 v2, v2, v3, s90 bitop3:0xa8\n"
    " v_cmp_ne_u32 vcc, 0, v2\n"
    " s_cbranch_vccz .Lsp_102\n"
    " s_sleep 1\n"
    " s_sub_u32 s57, s57, 1\n"
    " s_cbranch_scc0 .Lgs_100\n"
    " s_mov_b32 s57, 0\n"
    ".Lsp_102:\n"
    " s_branch .Lgsb_101\n"
    ".Lgs_103:\n"
    " buffer_load_dwordx4 v[60:63], v1, s[12:15], s78 offen offset:3072 sc1\n"
    " buffer_load_dwordx4 v[64:67], v1, s[12:15], s79 offen offset:0 sc1\n"
    " buffer_load_dwordx4 v[68:71], v1, s[12:15], s79 offen offset:1024 sc1\n"
    " buffer_load_dwordx4 v[72:75], v1, s[12:15], s79 offen offset:2048 sc1\n"
    " s_waitcnt vmcnt(0)\n"
    " v_bitop3_b32 v2, v60, v63, s88 bitop3:0x7e\n"
    " v_bitop3_b32 v3, v61, v62, s89 bitop3:0x7e\n"
    " v_bitop3_b32 v2, v2, v3, s90 bitop3:0xa8\n"
    " v_cmp_ne_u32 vcc, 0, v2\n"
    " s_cbranch_vccz .Lsp_105\n"
    " s_sleep 1\n"
    " s_sub_u32 s57, s57, 1\n"
    " s_cbranch_scc0 .Lgs_103\n"
    " s_mov_b32 s57, 0\n"
    ".Lsp_105:\n"
    " s_branch .Lgsb_104\n"
    ".Lgs_106:\n"
    " buffer_load_dwordx4 v[64:67], v1, s[12:15], s79 offen offset:0 sc1\n"
    " buffer_load_dwordx4 v[68:71], v1, s[12:15], s79 offen offset:1024 sc1\n"
    " buffer_load_dwordx4 v[72:75], v1, s[12:15], s79 offen offset:2048 sc1\n"
    " s_waitcnt vmcnt(0)\n"
    " v_bitop3_b32 v2, v64, v67, s88 bitop3:0x7e\n"
    " v_bitop3_b32 v3, v65, v66, s89 bitop3:0x7e\n"
    " v_bitop3_b32 v2, v2, v3, s90 bitop3:0xa8\n"
    " v_cmp_ne_u32 vcc, 0, v2\n"
    " s_cbranch_vccz .Lsp_108\n"
    " s_sleep 1\n"
    " s_sub_u32 s57, s57, 1\n"
    " s_cbranch_scc0 .Lgs_106\n"
    " s_mov_b32 s57, 0\n"
    ".Lsp_108:\n"
    " s_branch .Lgsb_107\n"
    ".Lgs_109:\n"
    " buffer_load_dwordx4 v[68:71], v1, s[12:15], s79 offen offset:1024 sc1\n"
    " buffer_load_dwordx4 v[72:75], v1, s[12:15], s79 offen offset:2048 sc1\n"
    " s_waitcnt vmcnt(0)\n"
    " v_bitop3_b32 v2, v68, v71, s88 bitop3:0x7e\n"
    " v_bitop3_b32 v3, v69, v70, s89 bitop3:0x7e\n"
    " v_bitop3_b32 v2, v2, v3, s90 bitop3:0xa8\n"
    " v_cmp_ne_u32 vcc, 0, v2\n"
    " s_cbranch_vccz .Lsp_111\n"
    " s_sleep 1\n"
    " s_sub_u32 s57, s57, 1\n"
    " s_cbranch_scc0 .Lgs_109\n"
    " s_mov_b32 s57, 0\n"
    ".Lsp_111:\n"
    " s_branch .Lgsb_110\n"
    ".Lgs_112:\n"
    " buffer_load_dwordx4 v[72:75], v1, s[12:15], s79 offen offset:2048 sc1\n"
    " s_waitcnt vmcnt(0)\n"
    " v_bitop3_b32 v2, v72, v75, s88 bitop3:0x7e\n"
    " v_bitop3_b32 v3, v73, v74, s89 bitop3:0x7e\n"
    " v_bitop3_b32 v2, v2, v3, s90 bitop3:0xa8\n"
    " v_cmp_ne_u32 vcc, 0, v2\n"
    " s_cbranch_vccz .Lsp_114\n"
    " s_sleep 1\n"
    " s_sub_u32 s57, s57, 1\n"
    " s_cbranch_scc0 .Lgs_112\n"
    " s_mov_b32 s57, 0\n"
    ".Lsp_114:\n"
    " s_branch .Lgsb_113\n"
    ".Lhs_115:\n"
    " buffer_load_dwordx4 v[8:11], v1, s[12:15], s69 offen offset:0 sc1\n"
    " buffer_load_dwordx4 v[12:15], v1, s[12:15], s69 offen offset:1024 sc1\n"
    " buffer_load_dwordx4 v[16:19], v1, s[12:15], s69 offen offset:2048 sc1\n"
    " buffer_load_dwordx4 v[20:23], v1, s[12:15], s69 offen offset:3072 sc1\n"
    " buffer_load_dwordx4 v[24:27], v1, s[12:15], s66 offen offset:0 sc1\n"
    " buffer_load_dwordx4 v[28:31], v1, s[12:15], s66 offen offset:1024 sc1\n"
    " buffer_load_dwordx4 v[32:35], v1, s[12:15], s66 offen offset:2048 sc1\n"
    " buffer_load_dwordx4 v[36:39], v1, s[12:15], s66 offen offset:3072 sc1\n"
    " s_waitcnt vmcnt(7)\n"
    " ds_write_b128 v7, v[8:11] offset:0\n"
    " v_max3_u32 v5, v8, v9, v10\n"
    " v_max3_u32 v5, v10, v11, v5\n"
    " s_waitcnt vmcnt(6)\n"
    " ds_write_b128 v7, v[12:15] offset:1024\n"
    " v_max3_u32 v5, v12, v13, v5\n"
    " v_max3_u32 v5, v14, v15, v5\n"
    " s_waitcnt vmcnt(5)\n"
    " ds_write_b128 v7, v[16:19] offset:2048\n"
    " v_max3_u32 v5, v16, v17, v5\n"
    " v_max3_u32 v5, v18, v19, v5\n"
    " s_waitcnt vmcnt(4)\n"
    " ds_write_b128 v7, v[20:23] offset:3072\n"
    " v_max3_u32 v5, v20, v21, v5\n"
    " v_max3_u32 v5, v22, v23, v5\n"
    " s_waitcnt vmcnt(3)\n"
    " ds_write_b128 v7, v[24:27] offset:4096\n"
    " v_max3_u32 v5, v24, v25, v5\n"
    " v_max3_u32 v5, v26, v27, v5\n"
    " s_waitcnt vmcnt(2)\n"
    " ds_write_b128 v7, v[28:31] offset:5120\n"
    " v_max3_u32 v5, v28, v29, v5\n"
    " v_max3_u32 v5, v30, v31, v5\n"
    " s_waitcnt vmcnt(1)\n"
    " ds_write_b128 v7, v[32:35] offset:6144\n"
    " v_max3_u32 v5, v32, v33, v5\n"
    " v_max3_u32 v5, v34, v35, v5\n"
    " s_waitcnt vmcnt(0)\n"
    " ds_write_b128 v7, v[36:39] offset:7168\n"
    " v_max3_u32 v5, v36, v37, v5\n"
    " v_max3_u32 v5, v38, v39, v5\n"
    " v_cmp_eq_u32 vcc, -1, v5\n"
    " s_cbranch_vccz .Lsp_117\n"
    " s_sub_u32 s57, s57, 1\n"
    " s_cbranch_scc0 .Lhs_115\n"
    " s_mov_b32 s57, 0\n"
    ".Lsp_117:\n"
    " s_branch .Lhsb_116\n"
    ".Lgs_118:\n"
    " buffer_load_dwordx4 v[172:175], v1, s[12:15], s72 offen offset:0 sc1\n"
    " buffer_load_dwordx4 v[176:179], v1, s[12:15], s72 offen offset:1024 sc1\n"
    " buffer_load_dwordx4 v[180:183], v1, s[12:15], s72 offen offset:2048 sc1\n"
    " buffer_load_dwordx4 v[184:187], v1, s[12:15], s72 offen offset:3072 sc1\n"
    " buffer_load_dwordx4 v[188:191], v1, s[12:15], s73 offen offset:0 sc1\n"
    " buffer_load_dwordx4 v[192:195], v1, s[12:15], s73 offen offset:1024 sc1\n"
    " buffer_load_dwordx4 v[196:199], v1, s[12:15], s73 offen offset:2048 sc1\n"
    " s_waitcnt vmcnt(0)\n"
    " v_bitop3_b32 v2, v172, v175, s88 bitop3:0x7e\n"
    " v_bitop3_b32 v3, v173, v174, s89 bitop3:0x7e\n"
    " v_bitop3_b32 v2, v2, v3, s90 bitop3:0xa8\n"
    " v_cmp_ne_u32 vcc, 0, v2\n"
    " s_cbranch_vccz .Lsp_120\n"
    " s_sleep 1\n"
    " s_sub_u32 s57, s57, 1\n"
    " s_cbranch_scc0 .Lgs_118\n"
    " s_mov_b32 s57, 0\n"
    ".Lsp_120:\n"
    " s_branch .Lgsb_119\n"
    ".Lgs_121:\n"
    " buffer_load_dwordx4 v[176:179], v1, s[12:15], s72 offen offset:1024 sc1\n"
    " buffer_load_dwordx4 v[180:183], v1, s[12:15], s72 offen offset:2048 sc1\n"
    " buffer_load_dwordx4 v[184:187], v1, s[12:15], s72 offen offset:3072 sc1\n"
    " buffer_load_dwordx4 v[188:191], v1, s[12:15], s73 offen offset:0 sc1\n"
    " buffer_load_dwordx4 v[192:195], v1, s[12:15], s73 offen offset:1024 sc1\n"
    " buffer_load_dwordx4 v[196:199], v1, s[12:15], s73 offen offset:2048 sc1\n"
    " buffer_load_dwordx4 v[232:235], v1, s[12:15], s74 offen offset:0 sc1\n"
    " s_waitcnt vmcnt(0)\n"
    " v_bitop3_b32 v2, v176, v179, s88 bitop3:0x7e\n"
    " v_bitop3_b32 v3, v177, v178, s89 bitop3:0x7e\n"
    " v_bitop3_b32 v2, v2, v3, s90 bitop3:0xa8\n"
    " v_cmp_ne_u32 vcc, 0, v2\n"
    " s_cbranch_vccz .Lsp_123\n"
    " s_sleep 1\n"
    " s_sub_u32 s57, s57, 1\n"
    " s_cbranch_scc0 .Lgs_121\n"
    " s_mov_b32 s57, 0\n"
    ".Lsp_123:\n"
    " s_branch .Lgsb_122\n"
    ".Lgs_124:\n"
    " buffer_load_dwordx4 v[180:183], v1, s[12:15], s72 offen offset:2048 sc1\n"
    " buffer_load_dwordx4 v[184:187], v1, s[12:15], s72 offen offset:3072 sc1\n"
    " buffer_load_dwordx4 v[188:191], v1, s[12:15], s73 offen offset:0 sc1\n"
    " buffer_load_dwordx4 v[192:195], v1, s[12:15], s73 offen offset:1024 sc1\n"
    " buffer_load_dwordx4 v[196:199], v1, s[12:15], s73 offen offset:2048 sc1\n"
    " buffer_load_dwordx4 v[232:235], v1, s[12:15], s74 offen offset:0 sc1\n"
    " buffer_load_dwordx4 v[236:239], v1, s[12:15], s74 offen offset:1024 sc1\n"
    " s_waitcnt vmcnt(0)\n"
    " v_bitop3_b32 v2, v180, v183, s88 bitop3:0x7e\n"
    " v_bitop3_b32 v3, v181, v182, s89 bitop3:0x7e\n"
    " v_bitop3_b32 v2, v2, v3, s90 bitop3:0xa8\n"
    " v_cmp_ne_u32 vcc, 0, v2\n"
    " s_cbranch_vccz .Lsp_126\n"
    " s_sleep 1\n"
    " s_sub_u32 s57, s57, 1\n"
    " s_cbranch_scc0 .Lgs_124\n"
    " s_mov_b32 s57, 0\n"
    ".Lsp_126:\n"
    " s_branch .Lgsb_125\n"
    ".Lgs_127:\n"
    " buffer_load_dwordx4 v[184:187], v1, s[12:15], s72 offen offset:3072 sc1\n"
    " buffer_load_dwordx4 v[188:191], v1, s[12:15], s73 offen offset:0 sc1\n"
    " buffer_load_dwordx4 v[192:195], v1, s[12:15], s73 offen offset:1024 sc1\n"
    " buffer_load_dwordx4 v[196:199], v1, s[12:15], s73 offen offset:2048 sc1\n"
    " buffer_load_dwordx4 v[232:235], v1, s[12:15], s74 offen offset:0 sc1\n"
    " buffer_load_dwordx4 v[236:239], v1, s[12:15], s74 offen offset:1024 sc1\n"
    " buffer_load_dwordx4 v[240:243], v1, s[12:15], s74 offen offset:2048 sc1\n"
    " s_waitcnt vmcnt(0)\n"
    " v_bitop3_b32 v2, v184, v187, s88 bitop3:0x7e\n"
    " v_bitop3_b32 v3, v185, v186, s89 bitop3:0x7e\n"
    " v_bitop3_b32 v2, v2, v3, s90 bitop3:0xa8\n"
    " v_cmp_ne_u32 vcc, 0, v2\n"
    " s_cbranch_vccz .Lsp_129\n"
    " s_sleep 1\n"
    " s_sub_u32 s57, s57, 1\n"
    " s_cbranch_scc0 .Lgs_127\n"
    " s_mov_b32 s57, 0\n"
    ".Lsp_129:\n"
    " s_branch .Lgsb_128\n"
    ".Lgs_130:\n"
    " buffer_load_dwordx4 v[188:191], v1, s[12:15], s73 offen offset:0 sc1\n"
    " buffer_load_dwordx4 v[192:195], v1, s[12:15], s73 offen offset:1024 sc1\n"
    " buffer_load_dwordx4 v[196:199], v1, s[12:15], s73 offen offset:2048 sc1\n"
    " buffer_load_dwordx4 v[232:235], v1, s[12:15], s74 offen offset:0 sc1\n"
    " buffer_load_dwordx4 v[236:239], v1, s[12:15], s74 offen offset:1024 sc1\n"
    " buffer_load_dwordx4 v[240:243], v1, s[12:15], s74 offen offset:2048 sc1\n"
    " buffer_load_dwordx4 v[244:247], v1, s[12:15], s74 offen offset:3072 sc1\n"
    " s_waitcnt vmcnt(0)\n"
    " v_bitop3_b32 v2, v188, v191, s88 bitop3:0x7e\n"
    " v_bitop3_b32 v3, v189, v190, s89 bitop3:0x7e\n"
    " v_bitop3_b32 v2, v2, v3, s90 bitop3:0xa8\n"
    " v_cmp_ne_u32 vcc, 0, v2\n"
    " s_cbranch_vccz .Lsp_132\n"
    " s_sleep 1\n"
    " s_sub_u32 s57, s57, 1\n"
    " s_cbranch_scc0 .Lgs_130\n"
    " s_mov_b32 s57, 0\n"
    ".Lsp_132:\n"
    " s_branch .Lgsb_131\n"
    ".Lgs_133:\n"
    " buffer_load_dwordx4 v[192:195], v1, s[12:15], s73 offen offset:1024 sc1\n"
    " buffer_load_dwordx4 v[196:199], v1, s[12:15], s73 offen offset:2048 sc1\n"
    " buffer_load_dwordx4 v[232:235], v1, s[12:15], s74 offen offset:0 sc1\n"
    " buffer_load_dwordx4 v[236:239], v1, s[12:15], s74 offen offset:1024 sc1\n"
    " buffer_load_dwordx4 v[240:243], v1, s[12:15], s74 offen offset:2048 sc1\n"
    " buffer_load_dwordx4 v[244:247], v1, s[12:15], s74 offen offset:3072 sc1\n"
    " buffer_load_dwordx4 v[248:251], v1, s[12:15], s75 offen offset:0 sc1\n"
    " s_waitcnt vmcnt(0)\n"
    " v_bitop3_b32 v2, v192, v195, s88 bitop3:0x7e\n"
    " v_bitop3_b32 v3, v193, v194, s89 bitop3:0x7e\n"
    " v_bitop3_b32 v2, v2, v3, s90 bitop3:0xa8\n"
    " v_cmp_ne_u32 vcc, 0, v2\n"
    " s_cbranch_vccz .Lsp_135\n"
    " s_sleep 1\n"
    " s_sub_u32 s57, s57, 1\n"
    " s_cbranch_scc0 .Lgs_133\n"
    " s_mov_b32 s57, 0\n"
    ".Lsp_135:\n"
    " s_branch .Lgsb_134\n"
    ".Lgs_136:\n"
    " buffer_load_dwordx4 v[196:199], v1, s[12:15], s73 offen offset:2048 sc1\n"
    " buffer_load_dwordx4 v[232:235], v1, s[12:15], s74 offen offset:0 sc1\n"
    " buffer_load_dwordx4 v[236:239], v1, s[12:15], s74 offen offset:1024 sc1\n"
    " buffer_load_dwordx4 v[240:243], v1, s[12:15], s74 offen offset:2048 sc1\n"
    " buffer_load_dwordx4 v[244:247], v1, s[12:15], s74 offen offset:3072 sc1\n"
    " buffer_load_dwordx4 v[248:251], v1, s[12:15], s75 offen offset:0 sc1\n"
    " buffer_load_dwordx4 v[252:255], v1, s[12:15], s75 offen offset:1024 sc1\n"
    " s_waitcnt vmcnt(0)\n"
    " v_bitop3_b32 v2, v196, v199, s88 bitop3:0x7e\n"
    " v_bitop3_b32 v3, v197, v198, s89 bitop3:0x7e\n"
    " v_bitop3_b32 v2, v2, v3, s90 bitop3:0xa8\n"
    " v_cmp_ne_u32 vcc, 0, v2\n"
    " s_cbranch_vccz .Lsp_138\n"
    " s_sleep 1\n"
    " s_sub_u32 s57, s57, 1\n"
    " s_cbranch_scc0 .Lgs_136\n"
    " s_mov_b32 s57, 0\n"
    ".Lsp_138:\n"
    " s_branch .Lgsb_137\n"
    ".Lgs_139:\n"
    " buffer_load_dwordx4 v[232:235], v1, s[12:15], s74 offen offset:0 sc1\n"
    " buffer_load_dwordx4 v[236:239], v1, s[12:15], s74 offen offset:1024 sc1\n"
    " buffer_load_dwordx4 v[240:243], v1, s[12:15], s74 offen offset:2048 sc1\n"
    " buffer_load_dwordx4 v[244:247], v1, s[12:15], s74 offen offset:3072 sc1\n"
    " buffer_load_dwordx4 v[248:251], v1, s[12:15], s75 offen offset:0 sc1\n"
    " buffer_load_dwordx4 v[252:255], v1, s[12:15], s75 offen offset:1024 sc1\n"
    " buffer_load_dwordx4 v[52:55], v1, s[12:15], s75 offen offset:2048 sc1\n"
    " s_waitcnt vmcnt(0)\n"
    " v_bitop3_b32 v2, v232, v235, s88 bitop3:0x7e\n"
    " v_bitop3_b32 v3, v233, v234, s89 bitop3:0x7e\n"
    " v_bitop3_b32 v2, v2, v3, s90 bitop3:0xa8\n"
    " v_cmp_ne_u32 vcc, 0, v2\n"
    " s_cbranch_vccz .Lsp_141\n"
    " s_sleep 1\n"
    " s_sub_u32 s57, s57, 1\n"
    " s_cbranch_scc0 .Lgs_139\n"
    " s_mov_b32 s57, 0\n"
    ".Lsp_141:\n"
    " s_branch .Lgsb_140\n"
    ".Lgs_142:\n"
    " buffer_load_dwordx4 v[236:239], v1, s[12:15], s74 offen offset:1024 sc1\n"
    " buffer_load_dwordx4 v[240:243], v1, s[12:15], s74 offen offset:2048 sc1\n"
    " buffer_load_dwordx4 v[244:247], v1, s[12:15], s74 offen offset:3072 sc1\n"
    " buffer_load_dwordx4 v[248:251], v1, s[12:15], s75 offen offset:0 sc1\n"
    " buffer_load_dwordx4 v[252:255], v1, s[12:15], s75 offen offset:1024 sc1\n"
    " buffer_load_dwordx4 v[52:55], v1, s[12:15], s75 offen offset:2048 sc1\n"
    " buffer_load_dwordx4 v[56:59], v1, s[12:15], s76 offen offset:0 sc1\n"
    " s_waitcnt vmcnt(0)\n"
    " v_bitop3_b32 v2, v236, v239, s88 bitop3:0x7e\n"
    " v_bitop3_b32 v3, v237, v238, s89 bitop3:0x7e\n"
    " v_bitop3_b32 v2, v2, v3, s90 bitop3:0xa8\n"
    " v_cmp_ne_u32 vcc, 0, v2\n"
    " s_cbranch_vccz .Lsp_144\n"
    " s_sleep 1\n"
    " s_sub_u32 s57, s57, 1\n"
    " s_cbranch_scc0 .Lgs_142\n"
    " s_mov_b32 s57, 0\n"
    ".Lsp_144:\n"
    " s_branch .Lgsb_143\n"
    ".Lgs_145:\n"
    " buffer_load_dwordx4 v[240:243], v1, s[12:15], s74 offen offset:2048 sc1\n"
    " buffer_load_dwordx4 v[244:247], v1, s[12:15], s74 offen offset:3072 sc1\n"
    " buffer_load_dwordx4 v[248:251], v1, s[12:15], s75 offen offset:0 sc1\n"
    " buffer_load_dwordx4 v[252:255], v1, s[12:15], s75 offen offset:1024 sc1\n"
    " buffer_load_dwordx4 v[52:55], v1, s[12:15], s75 offen offset:2048 sc1\n"
    " buffer_load_dwordx4 v[56:59], v1, s[12:15], s76 offen offset:0 sc1\n"
    " buffer_load_dwordx4 v[60:63], v1, s[12:15], s76 offen offset:1024 sc1\n"
    " s_waitcnt vmcnt(0)\n"
    " v_bitop3_b32 v2, v240, v243, s88 bitop3:0x7e\n"
    " v_bitop3_b32 v3, v241, v242, s89 bitop3:0x7e\n"
    " v_bitop3_b32 v2, v2, v3, s90 bitop3:0xa8\n"
    " v_cmp_ne_u32 vcc, 0, v2\n"
    " s_cbranch_vccz .Lsp_147\n"
    " s_sleep 1\n"
    " s_sub_u32 s57, s57, 1\n"
    " s_cbranch_scc0 .Lgs_145\n"
    " s_mov_b32 s57, 0\n"
    ".Lsp_147:\n"
    " s_branch .Lgsb_146\n"
    ".Lgs_148:\n"
    " buffer_load_dwordx4 v[244:247], v1, s[12:15], s74 offen offset:3072 sc1\n"
    " buffer_load_dwordx4 v[248:251], v1, s[12:15], s75 offen offset:0 sc1\n"
    " buffer_load_dwordx4 v[252:255], v1, s[12:15], s75 offen offset:1024 sc1\n"
    " buffer_load_dwordx4 v[52:55], v1, s[12:15], s75 offen offset:2048 sc1\n"
    " buffer_load_dwordx4 v[56:59], v1, s[12:15], s76 offen offset:0 sc1\n"
    " buffer_load_dwordx4 v[60:63], v1, s[12:15], s76 offen offset:1024 sc1\n"
    " buffer_load_dwordx4 v[64:67], v1, s[12:15], s76 offen offset:2048 sc1\n"
    " s_waitcnt vmcnt(0)\n"
    " v_bitop3_b32 v2, v244, v247, s88 bitop3:0x7e\n"
    " v_bitop3_b32 v3, v245, v246, s89 bitop3:0x7e\n"
    " v_bitop3_b32 v2, v2, v3, s90 bitop3:0xa8\n"
    " v_cmp_ne_u32 vcc, 0, v2\n"
    " s_cbranch_vccz .Lsp_150\n"
    " s_sleep 1\n"
    " s_sub_u32 s57, s57, 1\n"
    " s_cbranch_scc0 .Lgs_148\n"
    " s_mov_b32 s57, 0\n"
    ".Lsp_150:\n"
    " s_branch .Lgsb_149\n"
    ".Lgs_151:\n"
    " buffer_load_dwordx4 v[248:251], v1, s[12:15], s75 offen offset:0 sc1\n"
    " buffer_load_dwordx4 v[252:255], v1, s[12:15], s75 offen offset:1024 sc1\n"
    " buffer_load_dwordx4 v[52:55], v1, s[12:15], s75 offen offset:2048 sc1\n"
    " buffer_load_dwordx4 v[56:59], v1, s[12:15], s76 offen offset:0 sc1\n"
    " buffer_load_dwordx4 v[60:63], v1, s[12:15], s76 offen offset:1024 sc1\n"
    " buffer_load_dwordx4 v[64:67], v1, s[12:15], s76 offen offset:2048 sc1\n"
    " buffer_load_dwordx4 v[68:71], v1, s[12:15], s76 offen offset:3072 sc1\n"
    " s_waitcnt vmcnt(0)\n"
    " v_bitop3_b32 v2, v248, v251, s88 bitop3:0x7e\n"
    " v_bitop3_b32 v3, v249, v250, s89 bitop3:0x7e\n"
    " v_bitop3_b32 v2, v2, v3, s90 bitop3:0xa8\n"
    " v_cmp_ne_u32 vcc, 0, v2\n"
    " s_cbranch_vccz .Lsp_153\n"
    " s_sleep 1\n"
    " s_sub_u32 s57, s57, 1\n"
    " s_cbranch_scc0 .Lgs_151\n"
    " s_mov_b32 s57, 0\n"
    ".Lsp_153:\n"
    " s_branch .Lgsb_152\n"
    ".Lgs_154:\n"
    " buffer_load_dwordx4 v[252:255], v1, s[12:15], s75 offen offset:1024 sc1\n"
    " buffer_load_dwordx4 v[52:55], v1, s[12:15], s75 offen offset:2048 sc1\n"
    " buffer_load_dwordx4 v[56:59], v1, s[12:15], s76 offen offset:0 sc1\n"
    " buffer_load_dwordx4 v[60:63], v1, s[12:15], s76 offen offset:1024 sc1\n"
    " buffer_load_dwordx4 v[64:67], v1, s[12:15], s76 offen offset:2048 sc1\n"
    " buffer_load_dwordx4 v[68:71], v1, s[12:15], s76 offen offset:3072 sc1\n"
    " buffer_load_dwordx4 v[72:75], v1, s[12:15], s77 offen offset:0 sc1\n"
    " s_waitcnt vmcnt(0)\n"
    " v_bitop3_b32 v2, v252, v255, s88 bitop3:0x7e\n"
    " v_bitop3_b32 v3, v253, v254, s89 bitop3:0x7e\n"
    " v_bitop3_b32 v2, v2, v3, s90 bitop3:0xa8\n"
    " v_cmp_ne_u32 vcc, 0, v2\n"
    " s_cbranch_vccz .Lsp_156\n"
    " s_sleep 1\n"
    " s_sub_u32 s57, s57, 1\n"
    " s_cbranch_scc0 .Lgs_154\n"
    " s_mov_b32 s57, 0\n"
    ".Lsp_156:\n"
    " s_branch .Lgsb_155\n"
    ".Lgs_157:\n"
    " buffer_load_dwordx4 v[52:55], v1, s[12:15], s75 offen offset:2048 sc1\n"
    " buffer_load_dwordx4 v[56:59], v1, s[12:15], s76 offen offset:0 sc1\n"
    " buffer_load_dwordx4 v[60:63], v1, s[12:15], s76 offen offset:1024 sc1\n"
    " buffer_load_dwordx4 v[64:67], v1, s[12:15], s76 offen offset:2048 sc1\n"
    " buffer_load_dwordx4 v[68:71], v1, s[12:15], s76 offen offset:3072 sc1\n"
    " buffer_load_dwordx4 v[72:75], v1, s[12:15], s77 offen offset:0 sc1\n"
    " buffer_load_dwordx4 v[24:27], v1, s[12:15], s77 offen offset:1024 sc1\n"
    " s_waitcnt vmcnt(0)\n"
    " v_bitop3_b32 v2, v52, v55, s88 bitop3:0x7e\n"
    " v_bitop3_b32 v3, v53, v54, s89 bitop3:0x7e\n"
    " v_bitop3_b32 v2, v2, v3, s90 bitop3:0xa8\n"
    " v_cmp_ne_u32 vcc, 0, v2\n"
    " s_cbranch_vccz .Lsp_159\n"
    " s_sleep 1\n"
    " s_sub_u32 s57, s57, 1\n"
    " s_cbranch_scc0 .Lgs_157\n"
    " s_mov_b32 s57, 0\n"
    ".Lsp_159:\n"
    " s_branch .Lgsb_158\n"
    ".Lgs_160:\n"
    " buffer_load_dwordx4 v[56:59], v1, s[12:15], s76 offen offset:0 sc1\n"
    " buffer_load_dwordx4 v[60:63], v1, s[12:15], s76 offen offset:1024 sc1\n"
    " buffer_load_dwordx4 v[64:67], v1, s[12:15], s76 offen offset:2048 sc1\n"
    " buffer_load_dwordx4 v[68:71], v1, s[12:15], s76 offen offset:3072 sc1\n"
    " buffer_load_dwordx4 v[72:75], v1, s[12:15], s77 offen offset:0 sc1\n"
    " buffer_load_dwordx4 v[24:27], v1, s[12:15], s77 offen offset:1024 sc1\n"
    " buffer_load_dwordx4 v[28:31], v1, s[12:15], s77 offen offset:2048 sc1\n"
    " s_waitcnt vmcnt(0)\n"
    " v_bitop3_b32 v2, v56, v59, s88 bitop3:0x7e\n"
    " v_bitop3_b32 v3, v57, v58, s89 bitop3:0x7e\n"
    " v_bitop3_b32 v2, v2, v3, s90 bitop3:0xa8\n"
    " v_cmp_ne_u32 vcc, 0, v2\n"
    " s_cbranch_vccz .Lsp_162\n"
    " s_sleep 1\n"
    " s_sub_u32 s57, s57, 1\n"
    " s_cbranch_scc0 .Lgs_160\n"
    " s_mov_b32 s57, 0\n"
    ".Lsp_162:\n"
    " s_branch .Lgsb_161\n"
    ".Lgs_163:\n"
    " buffer_load_dwordx4 v[60:63], v1, s[12:15], s76 offen offset:1024 sc1\n"
    " buffer_load_dwordx4 v[64:67], v1, s[12:15], s76 offen offset:2048 sc1\n"
    " buffer_load_dwordx4 v[68:71], v1, s[12:15], s76 offen offset:3072 sc1\n"
    " buffer_load_dwordx4 v[72:75], v1, s[12:15], s77 offen offset:0 sc1\n"
    " buffer_load_dwordx4 v[24:27], v1, s[12:15], s77 offen offset:1024 sc1\n"
    " buffer_load_dwordx4 v[28:31], v1, s[12:15], s77 offen offset:2048 sc1\n"
    " buffer_load_dwordx4 v[32:35], v1, s[12:15], s78 offen offset:0 sc1\n"
    " s_waitcnt vmcnt(0)\n"
    " v_bitop3_b32 v2, v60, v63, s88 bitop3:0x7e\n"
    " v_bitop3_b32 v3, v61, v62, s89 bitop3:0x7e\n"
    " v_bitop3_b32 v2, v2, v3, s90 bitop3:0xa8\n"
    " v_cmp_ne_u32 vcc, 0, v2\n"
    " s_cbranch_vccz .Lsp_165\n"
    " s_sleep 1\n"
    " s_sub_u32 s57, s57, 1\n"
    " s_cbranch_scc0 .Lgs_163\n"
    " s_mov_b32 s57, 0\n"
    ".Lsp_165:\n"
    " s_branch .Lgsb_164\n"
    ".Lgs_166:\n"
    " buffer_load_dwordx4 v[64:67], v1, s[12:15], s76 offen offset:2048 sc1\n"
    " buffer_load_dwordx4 v[68:71], v1, s[12:15], s76 offen offset:3072 sc1\n"
    " buffer_load_dwordx4 v[72:75], v1, s[12:15], s77 offen offset:0 sc1\n"
    " buffer_load_dwordx4 v[24:27], v1, s[12:15], s77 offen offset:1024 sc1\n"
    " buffer_load_dwordx4 v[28:31], v1, s[12:15], s77 offen offset:2048 sc1\n"
    " buffer_load_dwordx4 v[32:35], v1, s[12:15], s78 offen offset:0 sc1\n"
    " buffer_load_dwordx4 v[36:39], v1, s[12:15], s78 offen offset:1024 sc1\n"
    " s_waitcnt vmcnt(0)\n"
    " v_bitop3_b32 v2, v64, v67, s88 bitop3:0x7e\n"
    " v_bitop3_b32 v3, v65, v66, s89 bitop3:0x7e\n"
    " v_bitop3_b32 v2, v2, v3, s90 bitop3:0xa8\n"
    " v_cmp_ne_u32 vcc, 0, v2\n"
    " s_cbranch_vccz .Lsp_168\n"
    " s_sleep 1\n"
    " s_sub_u32 s57, s57, 1\n"
    " s_cbranch_scc0 .Lgs_166\n"
    " s_mov_b32 s57, 0\n"
    ".Lsp_168:\n"
    " s_branch .Lgsb_167\n"
    ".Lgs_169:\n"
    " buffer_load_dwordx4 v[68:71], v1, s[12:15], s76 offen offset:3072 sc1\n"
    " buffer_load_dwordx4 v[72:75], v1, s[12:15], s77 offen offset:0 sc1\n"
    " buffer_load_dwordx4 v[24:27], v1, s[12:15], s77 offen offset:1024 sc1\n"
    " buffer_load_dwordx4 v[28:31], v1, s[12:15], s77 offen offset:2048 sc1\n"
    " buffer_load_dwordx4 v[32:35], v1, s[12:15], s78 offen offset:0 sc1\n"
    " buffer_load_dwordx4 v[36:39], v1, s[12:15], s78 offen offset:1024 sc1\n"
    " buffer_load_dwordx4 v[40:43], v1, s[12:15], s78 offen offset:2048 sc1\n"
    " s_waitcnt vmcnt(0)\n"
    " v_bitop3_b32 v2, v68, v71, s88 bitop3:0x7e\n"
    " v_bitop3_b32 v3, v69, v70, s89 bitop3:0x7e\n"
    " v_bitop3_b32 v2, v2, v3, s90 bitop3:0xa8\n"
    " v_cmp_ne_u32 vcc, 0, v2\n"
    " s_cbranch_vccz .Lsp_171\n"
    " s_sleep 1\n"
    " s_sub_u32 s57, s57, 1\n"
    " s_cbranch_scc0 .Lgs_169\n"
    " s_mov_b32 s57, 0\n"
    ".Lsp_171:\n"
    " s_branch .Lgsb_170\n"
    ".Lgs_172:\n"
    " buffer_load_dwordx4 v[72:75], v1, s[12:15], s77 offen offset:0 sc1\n"
    " buffer_load_dwordx4 v[24:27], v1, s[12:15], s77 offen offset:1024 sc1\n"
    " buffer_load_dwordx4 v[28:31], v1, s[12:15], s77 offen offset:2048 sc1\n"
    " buffer_load_dwordx4 v[32:35], v1, s[12:15], s78 offen offset:0 sc1\n"
    " buffer_load_dwordx4 v[36:39], v1, s[12:15], s78 offen offset:1024 sc1\n"
    " buffer_load_dwordx4 v[40:43], v1, s[12:15], s78 offen offset:2048 sc1\n"
    " buffer_load_dwordx4 v[44:47], v1, s[12:15], s78 offen offset:3072 sc1\n"
    " s_waitcnt vmcnt(0)\n"
    " v_bitop3_b32 v2, v72, v75, s88 bitop3:0x7e\n"
    " v_bitop3_b32 v3, v73, v74, s89 bitop3:0x7e\n"
    " v_bitop3_b32 v2, v2, v3, s90 bitop3:0xa8\n"
    " v_cmp_ne_u32 vcc, 0, v2\n"
    " s_cbranch_vccz .Lsp_174\n"
    " s_sleep 1\n"
    " s_sub_u32 s57, s57, 1\n"
    " s_cbranch_scc0 .Lgs_172\n"
    " s_mov_b32 s57, 0\n"
    ".Lsp_174:\n"
    " s_branch .Lgsb_173\n"
    ".Lgs_175:\n"
    " buffer_load_dwordx4 v[24:27], v1, s[12:15], s77 offen offset:1024 sc1\n"
    " buffer_load_dwordx4 v[28:31], v1, s[12:15], s77 offen offset:2048 sc1\n"
    " buffer_load_dwordx4 v[32:35], v1, s[12:15], s78 offen offset:0 sc1\n"
    " buffer_load_dwordx4 v[36:39], v1, s[12:15], s78 offen offset:1024 sc1\n"
    " buffer_load_dwordx4 v[40:43], v1, s[12:15], s78 offen offset:2048 sc1\n"
    " buffer_load_dwordx4 v[44:47], v1, s[12:15], s78 offen offset:3072 sc1\n"
    " buffer_load_dwordx4 v[172:175], v1, s[12:15], s79 offen offset:0 sc1\n"
    " s_waitcnt vmcnt(0)\n"
    " v_bitop3_b32 v2, v24, v27, s88 bitop3:0x7e\n"
    " v_bitop3_b32 v3, v25, v26, s89 bitop3:0x7e\n"
    " v_bitop3_b32 v2, v2, v3, s90 bitop3:0xa8\n"
    " v_cmp_ne_u32 vcc, 0, v2\n"
    " s_cbranch_vccz .Lsp_177\n"
    " s_sleep 1\n"
    " s_sub_u32 s57, s57, 1\n"
    " s_cbranch_scc0 .Lgs_175\n"
    " s_mov_b32 s57, 0\n"
    ".Lsp_177:\n"
    " s_branch .Lgsb_176\n"
    ".Lgs_178:\n"
    " buffer_load_dwordx4 v[28:31], v1, s[12:15], s77 offen offset:2048 sc1\n"
    " buffer_load_dwordx4 v[32:35], v1, s[12:15], s78 offen offset:0 sc1\n"
    " buffer_load_dwordx4 v[36:39], v1, s[12:15], s78 offen offset:1024 sc1\n"
    " buffer_load_dwordx4 v[40:43], v1, s[12:15], s78 offen offset:2048 sc1\n"
    " buffer_load_dwordx4 v[44:47], v1, s[12:15], s78 offen offset:3072 sc1\n"
    " buffer_load_dwordx4 v[172:175], v1, s[12:15], s79 offen offset:0 sc1\n"
    " buffer_load_dwordx4 v[176:179], v1, s[12:15], s79 offen offset:1024 sc1\n"
    " s_waitcnt vmcnt(0)\n"
    " v_bitop3_b32 v2, v28, v31, s88 bitop3:0x7e\n"
    " v_bitop3_b32 v3, v29, v30, s89 bitop3:0x7e\n"
    " v_bitop3_b32 v2, v2, v3, s90 bitop3:0xa8\n"
    " v_cmp_ne_u32 vcc, 0, v2\n"
    " s_cbranch_vccz .Lsp_180\n"
    " s_sleep 1\n"
    " s_sub_u32 s57, s57, 1\n"
    " s_cbranch_scc0 .Lgs_178\n"
    " s_mov_b32 s57, 0\n"
    ".Lsp_180:\n"
    " s_branch .Lgsb_179\n"
    ".Lgs_181:\n"
    " buffer_load_dwordx4 v[32:35], v1, s[12:15], s78 offen offset:0 sc1\n"
    " buffer_load_dwordx4 v[36:39], v1, s[12:15], s78 offen offset:1024 sc1\n"
    " buffer_load_dwordx4 v[40:43], v1, s[12:15], s78 offen offset:2048 sc1\n"
    " buffer_load_dwordx4 v[44:47], v1, s[12:15], s78 offen offset:3072 sc1\n"
    " buffer_load_dwordx4 v[172:175], v1, s[12:15], s79 offen offset:0 sc1\n"
    " buffer_load_dwordx4 v[176:179], v1, s[12:15], s79 offen offset:1024 sc1\n"
    " buffer_load_dwordx4 v[180:183], v1, s[12:15], s79 offen offset:2048 sc1\n"
    " s_waitcnt vmcnt(0)\n"
    " v_bitop3_b32 v2, v32, v35, s88 bitop3:0x7e\n"
    " v_bitop3_b32 v3, v33, v34, s89 bitop3:0x7e\n"
    " v_bitop3_b32 v2, v2, v3, s90 bitop3:0xa8\n"
    " v_cmp_ne_u32 vcc, 0, v2\n"
    " s_cbranch_vccz .Lsp_183\n"
    " s_sleep 1\n"
    " s_sub_u32 s57, s57, 1\n"
    " s_cbranch_scc0 .Lgs_181\n"
    " s_mov_b32 s57, 0\n"
    ".Lsp_183:\n"
    " s_branch .Lgsb_182\n"
    ".Lgs_184:\n"
    " buffer_load_dwordx4 v[36:39], v1, s[12:15], s78 offen offset:1024 sc1\n"
    " buffer_load_dwordx4 v[40:43], v1, s[12:15], s78 offen offset:2048 sc1\n"
    " buffer_load_dwordx4 v[44:47], v1, s[12:15], s78 offen offset:3072 sc1\n"
    " buffer_load_dwordx4 v[172:175], v1, s[12:15], s79 offen offset:0 sc1\n"
    " buffer_load_dwordx4 v[176:179], v1, s[12:15], s79 offen offset:1024 sc1\n"
    " buffer_load_dwordx4 v[180:183], v1, s[12:15], s79 offen offset:2048 sc1\n"
    " s_waitcnt vmcnt(0)\n"
    " v_bitop3_b32 v2, v36, v39, s88 bitop3:0x7e\n"
    " v_bitop3_b32 v3, v37, v38, s89 bitop3:0x7e\n"
    " v_bitop3_b32 v2, v2, v3, s90 bitop3:0xa8\n"
    " v_cmp_ne_u32 vcc, 0, v2\n"
    " s_cbranch_vccz .Lsp_186\n"
    " s_sleep 1\n"
    " s_sub_u32 s57, s57, 1\n"
    " s_cbranch_scc0 .Lgs_184\n"
    " s_mov_b32 s57, 0\n"
    ".Lsp_186:\n"
    " s_branch .Lgsb_185\n"
    ".Lgs_187:\n"
    " buffer_load_dwordx4 v[40:43], v1, s[12:15], s78 offen offset:2048 sc1\n"
    " buffer_load_dwordx4 v[44:47], v1, s[12:15], s78 offen offset:3072 sc1\n"
    " buffer_load_dwordx4 v[172:175], v1, s[12:15], s79 offen offset:0 sc1\n"
    " buffer_load_dwordx4 v[176:179], v1, s[12:15], s79 offen offset:1024 sc1\n"
    " buffer_load_dwordx4 v[180:183], v1, s[12:15], s79 offen offset:2048 sc1\n"
    " s_waitcnt vmcnt(0)\n"
    " v_bitop3_b32 v2, v40, v43, s88 bitop3:0x7e\n"
    " v_bitop3_b32 v3, v41, v42, s89 bitop3:0x7e\n"
    " v_bitop3_b32 v2, v2, v3, s90 bitop3:0xa8\n"
    " v_cmp_ne_u32 vcc, 0, v2\n"
    " s_cbranch_vccz .Lsp_189\n"
    " s_sleep 1\n"
    " s_sub_u32 s57, s57, 1\n"
    " s_cbranch_scc0 .Lgs_187\n"
    " s_mov_b32 s57, 0\n"
    ".Lsp_189:\n"
    " s_branch .Lgsb_188\n"
    ".Lgs_190:\n"
    " buffer_load_dwordx4 v[44:47], v1, s[12:15], s78 offen offset:3072 sc1\n"
    " buffer_load_dwordx4 v[172:175], v1, s[12:15], s79 offen offset:0 sc1\n"
    " buffer_load_dwordx4 v[176:179], v1, s[12:15], s79 offen offset:1024 sc1\n"
    " buffer_load_dwordx4 v[180:183], v1, s[12:15], s79 offen offset:2048 sc1\n"
    " s_waitcnt vmcnt(0)\n"
    " v_bitop3_b32 v2, v44, v47, s88 bitop3:0x7e\n"
    " v_bitop3_b32 v3, v45, v46, s89 bitop3:0x7e\n"
    " v_bitop3_b32 v2, v2, v3, s90 bitop3:0xa8\n"
    " v_cmp_ne_u32 vcc, 0, v2\n"
    " s_cbranch_vccz .Lsp_192\n"
    " s_sleep 1\n"
    " s_sub_u32 s57, s57, 1\n"
    " s_cbranch_scc0 .Lgs_190\n"
    " s_mov_b32 s57, 0\n"
    ".Lsp_192:\n"
    " s_branch .Lgsb_191\n"
    ".Lgs_193:\n"
    " buffer_load_dwordx4 v[172:175], v1, s[12:15], s79 offen offset:0 sc1\n"
    " buffer_load_dwordx4 v[176:179], v1, s[12:15], s79 offen offset:1024 sc1\n"
    " buffer_load_dwordx4 v[180:183], v1, s[12:15], s79 offen offset:2048 sc1\n"
    " s_waitcnt vmcnt(0)\n"
    " v_bitop3_b32 v2, v172, v175, s88 bitop3:0x7e\n"
    " v_bitop3_b32 v3, v173, v174, s89 bitop3:0x7e\n"
    " v_bitop3_b32 v2, v2, v3, s90 bitop3:0xa8\n"
    " v_cmp_ne_u32 vcc, 0, v2\n"
    " s_cbranch_vccz .Lsp_195\n"
    " s_sleep 1\n"
    " s_sub_u32 s57, s57, 1\n"
    " s_cbranch_scc0 .Lgs_193\n"
    " s_mov_b32 s57, 0\n"
    ".Lsp_195:\n"
    " s_branch .Lgsb_194\n"
    ".Lgs_196:\n"
    " buffer_load_dwordx4 v[176:179], v1, s[12:15], s79 offen offset:1024 sc1\n"
    " buffer_load_dwordx4 v[180:183], v1, s[12:15], s79 offen offset:2048 sc1\n"
    " s_waitcnt vmcnt(0)\n"
    " v_bitop3_b32 v2, v176, v179, s88 bitop3:0x7e\n"
    " v_bitop3_b32 v3, v177, v178, s89 bitop3:0x7e\n"
    " v_bitop3_b32 v2, v2, v3, s90 bitop3:0xa8\n"
    " v_cmp_ne_u32 vcc, 0, v2\n"
    " s_cbranch_vccz .Lsp_198\n"
    " s_sleep 1\n"
    " s_sub_u32 s57, s57, 1\n"
    " s_cbranch_scc0 .Lgs_196\n"
    " s_mov_b32 s57, 0\n"
    ".Lsp_198:\n"
    " s_branch .Lgsb_197\n"
    ".Lgs_199:\n"
    " buffer_load_dwordx4 v[180:183], v1, s[12:15], s79 offen offset:2048 sc1\n"
    " s_waitcnt vmcnt(0)\n"
    " v_bitop3_b32 v2, v180, v183, s88 bitop3:0x7e\n"
    " v_bitop3_b32 v3, v181, v182, s89 bitop3:0x7e\n"
    " v_bitop3_b32 v2, v2, v3, s90 bitop3:0xa8\n"
    " v_cmp_ne_u32 vcc, 0, v2\n"
    " s_cbranch_vccz .Lsp_201\n"
    " s_sleep 1\n"
    " s_sub_u32 s57, s57, 1\n"
    " s_cbranch_scc0 .Lgs_199\n"
    " s_mov_b32 s57, 0\n"
    ".Lsp_201:\n"
    " s_branch .Lgsb_200\n"
    ".Lend:\n"
    :
    : "{s[0:1]}"(__builtin_amdgcn_kernarg_segment_ptr()), "{s2}"(blockIdx.x), "{v0}"(threadIdx.x), "i"((__attribute__((address_space(3))) char*)lds)
    : "v1", "v2", "v3", "v4", "v5", "v6", "v7", "v8", "v9", "v10", "v11", "v12", "v13", "v14", "v15", "v16", "v17", "v18", "v19", "v20", "v21", "v22", "v23", "v24", "v25", "v26", "v27", "v28", "v29", "v30", "v31", "v32", "v33", "v34", "v35", "v36", "v37", "v38", "v39", "v40", "v41", "v42", "v43", "v44", "v45", "v46", "v47", "v48", "v49", "v50", "v51", "v52", "v53", "v54", "v55", "v56", "v57", "v58", "v59", "v60", "v61", "v62", "v63", "v64", "v65", "v66", "v67", "v68", "v69", "v70", "v71", "v72", "v73", "v74", "v75", "v76", "v77", "v78", "v79", "v80", "v81", "v82", "v83", "v84", "v85", "v86", "v87", "v88", "v89", "v90", "v91", "v92", "v93", "v94", "v95", "v96", "v97", "v98", "v99", "v100", "v101", "v102", "v103", "v104", "v105", "v106", "v107", "v108", "v109", "v110", "v111", "v112", "v113", "v114", "v115", "v116", "v117", "v118", "v119", "v120", "v121", "v122", "v123", "v124", "v125", "v126", "v127", "v128", "v129", "v130", "v131", "v132", "v133", "v134", "v135", "v136", "v137", "v138", "v139", "v140", "v141", "v142", "v143", "v144", "v145", "v146", "v147", "v148", "v149", "v150", "v151", "v152", "v153", "v154", "v155", "v156", "v157", "v158", "v159", "v160", "v161", "v162", "v163", "v164", "v165", "v166", "v167", "v168", "v169", "v170", "v171", "v172", "v173", "v174", "v175", "v176", "v177", "v178", "v179", "v180", "v181", "v182", "v183", "v184", "v185", "v186", "v187", "v188", "v189", "v190", "v191", "v192", "v193", "v194", "v195", "v196", "v197", "v198", "v199", "v200", "v201", "v202", "v203", "v204", "v205", "v206", "v207", "v208", "v209", "v210", "v211", "v212", "v213", "v214", "v215", "v216", "v217", "v218", "v219", "v220", "v221", "v222", "v223", "v224", "v225", "v226", "v227", "v228", "v229", "v230", "v231", "v232", "v233", "v234", "v235", "v236", "v237", "v238", "v239", "v240", "v241", "v242", "v243", "v244", "v245", "v246", "v247", "v248", "v249", "v250", "v251", "v252", "v253", "v254", "v255", "s3", "s4", "s5", "s6", "s7", "s8", "s9", "s10", "s11", "s12", "s13", "s14", "s15", "s16", "s17", "s18", "s19", "s20", "s21", "s22", "s23", "s24", "s25", "s26", "s27", "s28", "s29", "s30", "s31", "s33", "s34", "s35", "s36", "s37", "s38", "s39", "s40", "s41", "s42", "s43", "s44", "s45", "s46", "s47", "s48", "s49", "s50", "s51", "s52", "s53", "s54", "s55", "s56", "s57", "s58", "s59", "s60", "s61", "s62", "s63", "s64", "s65", "s66", "s67", "s68", "s69", "s70", "s71", "s72", "s73", "s74", "s75", "s76", "s77", "s78", "s79", "s80", "s81", "s82", "s83", "s84", "s85", "s86", "s87", "s88", "s89", "s90", "s91", "s92", "s93", "s94", "s95", "s96", "s97", "s98", "s99", "vcc", "scc", "memory");
}

extern "C" void kernel_launch(void* const* d_in, const int* in_sizes, int n_in, void* d_out, int out_size, void* d_ws,
                              size_t ws_size, hipStream_t stream) {
  constexpr uint32_t F = 0x00020000u, SD = 2048u * 512u * 2u, W = 512u * 1792u * 2u;
  const size_t WS_OFF = 0u << 20;
  Args a = {{d_in[1], SD, F}, {d_in[2], SD, F}, {(char*)d_ws + WS_OFF, 18350080u, F}, {d_in[3], 4096u, F}, {d_in[4], SD, F},
            {d_in[0], SD, F}, {d_in[7], W, F}, {d_in[8], W, F}, {d_out, SD, F}, {d_in[5], 1024u, F}};
  h3_fused<<<256, 512, 0, stream>>>(a);
}
```
